```python
import torch

def _softmax(x, axis=-1):
    return torch.softmax(x, dim=axis)
def _gelu(x):
    return torch.nn.functional.gelu(x, approximate="tanh")
def _silu(x):
    return torch.nn.functional.silu(x)
def _sigmoid(x):
    return torch.sigmoid(x)
def _layernorm(x, axis=-1, eps=1e-5):
    m = x.mean(dim=axis, keepdim=True)
    v = x.var(dim=axis, keepdim=True, unbiased=False)
    return (x - m) / torch.sqrt(v + eps)
def _scatter_add(tgt, idx, src, axis=0):
    return tgt.clone().index_add_(axis, idx, src)
def _segment_sum(src, seg, n):
    out = torch.zeros((n,) + src.shape[1:], dtype=src.dtype)
    return out.index_add_(0, seg, src)
def _pad(x, pw):
    flat = tuple(v for pair in reversed(pw) for v in pair)
    return torch.nn.functional.pad(x, flat)

kernel_name = 'parallel_sum__linear_kernel_hedgehog__dense_activation_silu__prompt'

def setup_inputs():

    _w = (lambda a: (lambda t: t.to(torch.bfloat16) if t.is_floating_point() else t)(torch.as_tensor(a)))
    def _A_setup():
        B, S, D, H = 1, 2048, 576, 32
        Dh = D // H
        return {
            "x": _w((lambda *s: torch.randn(*s, dtype=torch.bfloat16) * 0.05)(B, S, D)),
            "wqkv": _w((lambda *s: torch.randn(*s, dtype=torch.bfloat16) * 0.2)(D, 3 * D)),
            "w_phi": _w((lambda *s: torch.randn(*s, dtype=torch.bfloat16) * 0.2)(H, Dh, Dh) * (Dh ** -0.5)),
            "wo": _w((lambda *s: torch.randn(*s, dtype=torch.bfloat16) * 0.05)(D, D)),
        }
    _A_in = _A_setup()

    def _B_setup():
        B, S, D, F = 1, 2048, 576, 4608
        return {
            "x": _w((lambda *s: torch.randn(*s, dtype=torch.bfloat16) * 0.05)(B, S, D)),
            "w1": _w((lambda *s: torch.randn(*s, dtype=torch.bfloat16) * 1.6)(D, F) * (D ** -0.5)),
            "w2": _w((lambda *s: torch.randn(*s, dtype=torch.bfloat16) * 0.1)(F, D) * (F ** -0.5)),
        }
    _B_in = _B_setup()

    return {
        "A_x": _A_in["x"],
        "A_wqkv": _A_in["wqkv"],
        "A_w_phi": _A_in["w_phi"],
        "A_wo": _A_in["wo"],
        "B_x": _B_in["x"],
        "B_w1": _B_in["w1"],
        "B_w2": _B_in["w2"],
    }

def reference(A_x, A_wqkv, A_w_phi, A_wo, B_x, B_w1, B_w2):
    def _A_ref(x, wqkv, w_phi, wo):
        B, S, D = x.shape
        H = 32; Dh = D // H
        qkv = (lambda x, *d: x.permute(*d))((x @ wqkv).reshape(B, S, 3, H, Dh), 2, 0, 3, 1, 4)
        q, k, v = qkv[0], qkv[1], qkv[2]
        pq = torch.einsum("bhsd,hdf->bhsf", q, w_phi)
        pk = torch.einsum("bhsd,hdf->bhsf", k, w_phi)
        phi_q = torch.concatenate([_softmax(pq, axis=-1), _softmax(-pq, axis=-1)], axis=-1)
        phi_k = torch.concatenate([_softmax(pk, axis=-1), _softmax(-pk, axis=-1)], axis=-1)
        kv = torch.einsum("bhsf,bhsd->bhfd", phi_k, v)
        z  = torch.einsum("bhsf->bhf", phi_k)
        num = torch.einsum("bhqf,bhfd->bhqd", phi_q, kv)
        den = torch.einsum("bhqf,bhf->bhq", phi_q, z)[..., None] + 1e-6
        o = num / den
        return (lambda x, *d: x.permute(*d))(o, 0, 2, 1, 3).reshape(B, S, D) @ wo

    def _B_ref(x, w1, w2):
        h = torch.einsum("bsd,df->bsf", x, w1)
        h = _silu(h)
        out = torch.einsum("bsf,fd->bsd", h, w2)
        return out

    x = A_x
    xn = _layernorm(x, axis=-1)
    return x + _A_ref(xn, A_wqkv, A_w_phi, A_wo) + _B_ref(xn, B_w1, B_w2)
```

```cpp
#include <hip/hip_runtime.h>
#include <hip/hip_bf16.h>
#include <stdint.h>

typedef __attribute__((ext_vector_type(8))) short bf16x8;
typedef __attribute__((ext_vector_type(4))) float f32x4;
typedef unsigned short u16;

__device__ __forceinline__ float bf2f(u16 v) { return __uint_as_float(((unsigned)v) << 16); }
__device__ __forceinline__ u16 f2bf(float f) {
  unsigned u = __float_as_uint(f);
  u += 0x7fffu + ((u >> 16) & 1u);
  return (u16)(u >> 16);
}

constexpr int S = 2048, D = 576, H = 32, DH = 18, F = 4608;

__global__ __launch_bounds__(256) void k_ln(const u16* __restrict__ x, u16* __restrict__ xn) {
  int row = blockIdx.x * 4 + (threadIdx.x >> 6), lane = threadIdx.x & 63;
  const u16* xr = x + (size_t)row * D;
  float v[9]; float s = 0.f;
  for (int i = 0; i < 9; ++i) { v[i] = bf2f(xr[lane + 64 * i]); s += v[i]; }
  for (int o = 32; o; o >>= 1) s += __shfl_xor(s, o);
  float m = s * (1.f / D);
  float q = 0.f;
  for (int i = 0; i < 9; ++i) { float d = v[i] - m; q += d * d; }
  for (int o = 32; o; o >>= 1) q += __shfl_xor(q, o);
  float r = rsqrtf(q * (1.f / D) + 1e-5f);
  for (int i = 0; i < 9; ++i) xn[(size_t)row * D + lane + 64 * i] = f2bf((v[i] - m) * r);
}

template <int EPI>
__global__ __launch_bounds__(256) void k_gemm(const u16* __restrict__ A, int lda, const u16* __restrict__ B, int ldb,
                                              u16* __restrict__ C, float* __restrict__ Cf, int ldc, int K) {
  __shared__ u16 sA[64][40];
  __shared__ u16 sB[64][40];
  int bm = blockIdx.y * 64, bn = blockIdx.x * 64;
  int tid = threadIdx.x, wid = tid >> 6, lane = tid & 63, fr = lane & 15, fq = lane >> 4;
  int wr = wid >> 1, wc = wid & 1;
  f32x4 acc[2][2] = {};
  for (int k0 = 0; k0 < K; k0 += 32) {
    for (int e = tid; e < 64 * 32; e += 256) {
      int r = e >> 5, c = e & 31;
      sA[r][c] = A[(size_t)(bm + r) * lda + k0 + c];
      int kk = e >> 6, n = e & 63;
      sB[n][kk] = B[(size_t)(k0 + kk) * ldb + bn + n];
    }
    __syncthreads();
    bf16x8 a[2], b[2];
    for (int m = 0; m < 2; ++m) a[m] = *(const bf16x8*)&sA[wr * 32 + m * 16 + fr][fq * 8];
    for (int n = 0; n < 2; ++n) b[n] = *(const bf16x8*)&sB[wc * 32 + n * 16 + fr][fq * 8];
    for (int m = 0; m < 2; ++m) for (int n = 0; n < 2; ++n)
      acc[m][n] = __builtin_amdgcn_mfma_f32_16x16x32_bf16(a[m], b[n], acc[m][n], 0, 0, 0);
    __syncthreads();
  }
  for (int m = 0; m < 2; ++m) for (int n = 0; n < 2; ++n) for (int j = 0; j < 4; ++j) {
    int r = bm + wr * 32 + m * 16 + fq * 4 + j, c = bn + wc * 32 + n * 16 + fr;
    float v = acc[m][n][j];
    if (EPI == 0) C[(size_t)r * ldc + c] = f2bf(v);
    else if (EPI == 1) { float hb = bf2f(f2bf(v)); C[(size_t)r * ldc + c] = f2bf(hb / (1.f + __expf(-hb))); }
    else Cf[(size_t)r * ldc + c] = v;
  }
}

__global__ __launch_bounds__(256) void k_feat(const u16* __restrict__ qkv, const u16* __restrict__ wphi, u16* __restrict__ phi) {
  int idx = blockIdx.x * 256 + threadIdx.x;
  int which = idx & 1, h = (idx >> 1) & 31, row = idx >> 6;
  const u16* src = qkv + (size_t)row * (3 * D) + which * D + h * DH;
  float xq[DH]; for (int d = 0; d < DH; ++d) xq[d] = bf2f(src[d]);
  float p[DH];
  for (int f = 0; f < DH; ++f) { float s = 0.f; for (int d = 0; d < DH; ++d) s += xq[d] * bf2f(wphi[(h * DH + d) * DH + f]); p[f] = bf2f(f2bf(s)); }
  float mx = p[0], mn = p[0];
  for (int f = 1; f < DH; ++f) { mx = fmaxf(mx, p[f]); mn = fminf(mn, p[f]); }
  float e1[DH], e2[DH], s1 = 0.f, s2 = 0.f;
  for (int f = 0; f < DH; ++f) { e1[f] = __expf(p[f] - mx); s1 += e1[f]; e2[f] = __expf(mn - p[f]); s2 += e2[f]; }
  u16* dst = phi + ((size_t)which * S + row) * (H * 36) + h * 36;
  for (int f = 0; f < DH; ++f) { dst[f] = f2bf(e1[f] / s1); dst[18 + f] = f2bf(e2[f] / s2); }
}

__global__ __launch_bounds__(704) void k_kv(const u16* __restrict__ phi, const u16* __restrict__ qkv, float* __restrict__ kv) {
  int h = blockIdx.x, t = threadIdx.x; if (t >= 36 * 19) return;
  int f = t / 19, d = t % 19;
  const u16* pk = phi + (size_t)S * (H * 36) + h * 36 + f;
  const u16* v = qkv + 2 * D + h * DH + d;
  float s = 0.f;
  for (int r = 0; r < S; ++r) s += bf2f(pk[(size_t)r * (H * 36)]) * (d < 18 ? bf2f(v[(size_t)r * 3 * D]) : 1.f);
  kv[(h * 36 + f) * 19 + d] = bf2f(f2bf(s));
}

__global__ __launch_bounds__(256) void k_o(const u16* __restrict__ phi, const float* __restrict__ kv, u16* __restrict__ o) {
  int idx = blockIdx.x * 256 + threadIdx.x;
  int h = idx & 31, row = idx >> 5;
  const u16* pq = phi + (size_t)row * (H * 36) + h * 36;
  float num[19]; for (int d = 0; d < 19; ++d) num[d] = 0.f;
  for (int f = 0; f < 36; ++f) { float p = bf2f(pq[f]); for (int d = 0; d < 19; ++d) num[d] += p * kv[(h * 36 + f) * 19 + d]; }
  float den = bf2f(f2bf(num[18])) + 1e-6f;
  for (int d = 0; d < 18; ++d) o[(size_t)row * D + h * DH + d] = f2bf(bf2f(f2bf(num[d])) / den);
}

__global__ __launch_bounds__(256) void k_final(const u16* __restrict__ x, const float* __restrict__ a, const float* __restrict__ b, u16* __restrict__ out, int n) {
  int i = blockIdx.x * 256 + threadIdx.x; if (i >= n) return;
  out[i] = f2bf(bf2f(x[i]) + a[i] + b[i]);
}

extern "C" void kernel_launch(void* const* d_in, const int* in_sizes, int n_in, void* d_out, int out_size, void* d_ws, size_t ws_size, hipStream_t stream) {
  const u16* x = (const u16*)d_in[0]; const u16* wqkv = (const u16*)d_in[1]; const u16* wphi = (const u16*)d_in[2]; const u16* wo = (const u16*)d_in[3];
  const u16* w1 = (const u16*)d_in[5]; const u16* w2 = (const u16*)d_in[6];
  u16* out = (u16*)d_out;
  char* ws = (char*)d_ws;
  u16* xn = (u16*)ws; ws += (size_t)S * D * 2;
  u16* qkv = (u16*)ws; ws += (size_t)S * 3 * D * 2;
  u16* hbuf = (u16*)ws; ws += (size_t)S * F * 2;
  u16* phi = (u16*)ws; ws += (size_t)2 * S * H * 36 * 2;
  float* kv = (float*)ws; ws += 32 * 36 * 19 * 4 + 1024;
  u16* o = (u16*)ws; ws += (size_t)S * D * 2;
  float* oa = (float*)ws; ws += (size_t)S * D * 4;
  float* ob = (float*)ws; ws += (size_t)S * D * 4;
  k_ln<<<S / 4, 256, 0, stream>>>(x, xn);
  k_gemm<0><<<dim3(3 * D / 64, S / 64), 256, 0, stream>>>(xn, D, wqkv, 3 * D, qkv, nullptr, 3 * D, D);
  k_gemm<1><<<dim3(F / 64, S / 64), 256, 0, stream>>>(xn, D, w1, F, hbuf, nullptr, F, D);
  k_feat<<<S * H * 2 / 256, 256, 0, stream>>>(qkv, wphi, phi);
  k_kv<<<H, 704, 0, stream>>>(phi, qkv, kv);
  k_o<<<S * H / 256, 256, 0, stream>>>(phi, kv, o);
  k_gemm<2><<<dim3(D / 64, S / 64), 256, 0, stream>>>(o, D, wo, D, nullptr, oa, D, D);
  k_gemm<2><<<dim3(D / 64, S / 64), 256, 0, stream>>>(hbuf, F, w2, D, nullptr, ob, D, F);
  k_final<<<(S * D + 255) / 256, 256, 0, stream>>>(x, oa, ob, out, S * D);
}
```

```cpp
#include <hip/hip_runtime.h>
#include <hip/hip_bf16.h>
#include <stdint.h>

typedef __attribute__((ext_vector_type(8))) short bf16x8;
typedef __attribute__((ext_vector_type(4))) short s16x4;
typedef __attribute__((ext_vector_type(4))) float f32x4;
typedef unsigned short u16;
#define LDS_AS __attribute__((address_space(3)))

__device__ __forceinline__ float bf2f(u16 v) { return __uint_as_float(((unsigned)v) << 16); }
__device__ __forceinline__ u16 f2bf(float f) {
  unsigned u = __float_as_uint(f);
  u += 0x7fffu + ((u >> 16) & 1u);
  return (u16)(u >> 16);
}
__device__ __forceinline__ unsigned pack2(float lo, float hi) { return (unsigned)f2bf(lo) | ((unsigned)f2bf(hi) << 16); }

constexpr int S = 2048, D = 576, H = 32, DH = 18, F = 4608;

__global__ __launch_bounds__(256) void k_ln(const u16* __restrict__ x, u16* __restrict__ xn) {
  int row = blockIdx.x * 4 + (threadIdx.x >> 6), lane = threadIdx.x & 63;
  const u16* xr = x + (size_t)row * D;
  float v[9]; float s = 0.f;
  for (int i = 0; i < 9; ++i) { v[i] = bf2f(xr[lane + 64 * i]); s += v[i]; }
  for (int o = 32; o; o >>= 1) s += __shfl_xor(s, o);
  float m = s * (1.f / D);
  float q = 0.f;
  for (int i = 0; i < 9; ++i) { float d = v[i] - m; q += d * d; }
  for (int o = 32; o; o >>= 1) q += __shfl_xor(q, o);
  float r = rsqrtf(q * (1.f / D) + 1e-5f);
  for (int i = 0; i < 9; ++i) xn[(size_t)row * D + lane + 64 * i] = f2bf((v[i] - m) * r);
}

#define GLDS(g, l) __builtin_amdgcn_global_load_lds((const unsigned*)(g), (unsigned*)(l), 16, 0, 0)
#define WAIT_V(n) asm volatile("s_waitcnt vmcnt(" #n ")" ::: "memory")
#define WAIT_L0() asm volatile("s_waitcnt lgkmcnt(0)" ::: "memory")
#define BAR() __builtin_amdgcn_s_barrier()

constexpr int L1_STAGE = 40960;

__device__ __forceinline__ bf16x8 tr_frag(const char* p) {
  typedef LDS_AS s16x4* lp;
  s16x4 lo = __builtin_amdgcn_ds_read_tr16_b64_v4i16((lp)(p));
  s16x4 hi = __builtin_amdgcn_ds_read_tr16_b64_v4i16((lp)(p + 4096));
  return __builtin_shufflevector(lo, hi, 0, 1, 2, 3, 4, 5, 6, 7);
}

template <int TYPE>
__device__ __forceinline__ void l1_wg(char* smem, const u16* __restrict__ xn, const u16* __restrict__ w1, const u16* __restrict__ wqkv,
                                      const u16* __restrict__ wphi, u16* __restrict__ hbuf, u16* __restrict__ qb, float* __restrict__ kvslab,
                                      int rt, int ct) {
  constexpr int NCH = TYPE ? 36 : 54;
  constexpr int NTW = TYPE ? 5 : 7;
  const int tid = threadIdx.x, lane = tid & 63;
  const int wid = __builtin_amdgcn_readfirstlane(tid >> 6);
  const int wr = wid >> 2, wc = wid & 3, fr = lane & 15, fq = lane >> 4;
  const u16* ag = xn + (size_t)(rt * 128 + wid * 16 + (lane >> 2)) * D + (((lane & 3) ^ ((-(lane >> 4)) & 3)) * 8);
  const u16* bg[4]; int bst[4]; bool bval[4];
#pragma unroll
  for (int i = 0; i < 4; ++i) {
    int r = wid * 4 + i;
    int f = 2 * (i | (((wid >> 1) & 1) << 2));
    int lc = lane ^ f;
    bval[i] = lc < NCH;
    int lcc = bval[i] ? lc : 0;
    if (TYPE == 0) {
      int gc = ct * 432 + lcc * 8;
      if (gc < F) { bg[i] = w1 + (size_t)r * F + gc; bst[i] = 32 * F; }
      else { bg[i] = wqkv + (size_t)r * (3 * D) + (gc - F); bst[i] = 32 * 3 * D; }
    } else {
      int part = lcc >= 18 ? 1 : 0, cc = lcc - 18 * part;
      bg[i] = wqkv + (size_t)r * (3 * D) + D + part * D + ct * 144 + cc * 8; bst[i] = 32 * 3 * D;
    }
  }
  auto issue = [&](int sb_off) {
    char* sb = smem + sb_off;
    GLDS(ag, sb + wid * 1024);
    ag += 32;
#pragma unroll
    for (int i = 0; i < 4; ++i) {
      if (bval[i]) GLDS(bg[i], sb + 8192 + (wid * 4 + i) * 1024);
      bg[i] += bst[i];
    }
  };
  const int a_base = (wr * 64 + fr) * 64 + ((fq ^ ((-(fr >> 2)) & 3)) * 16);
  int b_off[NTW];
#pragma unroll
  for (int j = 0; j < NTW; ++j) {
    int c = 2 * (NTW * wc + j) + ((lane & 3) >> 1);
    int f = 2 * ((fr >> 2) | ((fq & 1) << 2));
    b_off[j] = 8192 + (8 * fq + (fr >> 2)) * 1024 + ((c ^ f) * 16) + 8 * (lane & 1);
  }
  f32x4 acc[4][NTW];
#pragma unroll
  for (int m = 0; m < 4; ++m)
#pragma unroll
    for (int j = 0; j < NTW; ++j) acc[m][j] = f32x4{0.f, 0.f, 0.f, 0.f};
  bf16x8 af[4], bf[NTW];
  issue(0); issue(L1_STAGE); issue(2 * L1_STAGE); issue(3 * L1_STAGE);
  WAIT_V(15); BAR();
#pragma unroll
  for (int m = 0; m < 4; ++m) af[m] = *(const bf16x8*)(smem + a_base + m * 1024);
#pragma unroll
  for (int j = 0; j < NTW; ++j) bf[j] = tr_frag(smem + b_off[j]);
#pragma unroll 1
  for (int t = 0; t < 17; ++t) {
    if (t < 15) WAIT_V(10); else if (t == 15) WAIT_V(5); else WAIT_V(0);
    WAIT_L0();
    BAR();
    if (t + 4 < 18) issue((t & 3) * L1_STAGE);
    const char* nb = smem + ((t + 1) & 3) * L1_STAGE;
#pragma unroll
    for (int j = 0; j < NTW; ++j) {
#pragma unroll
      for (int m = 0; m < 4; ++m)
        acc[m][j] = __builtin_amdgcn_mfma_f32_16x16x32_bf16(bf[j], af[m], acc[m][j], 0, 0, 0);
      bf[j] = tr_frag(nb + b_off[j]);
    }
#pragma unroll
    for (int m = 0; m < 4; ++m) af[m] = *(const bf16x8*)(nb + a_base + m * 1024);
  }
#pragma unroll
  for (int j = 0; j < NTW; ++j)
#pragma unroll
    for (int m = 0; m < 4; ++m)
      acc[m][j] = __builtin_amdgcn_mfma_f32_16x16x32_bf16(bf[j], af[m], acc[m][j], 0, 0, 0);
  if (TYPE == 0) {
#pragma unroll
    for (int m = 0; m < 4; ++m)
#pragma unroll
      for (int j = 0; j < NTW; ++j) {
        int T = NTW * wc + j;
        if (T < 27) {
          int row = rt * 128 + wr * 64 + m * 16 + fr;
          int col0 = ct * 432 + T * 16 + fq * 4;
          f32x4 v = acc[m][j];
          if (col0 < F) {
            float o[4];
#pragma unroll
            for (int e = 0; e < 4; ++e) o[e] = v[e] * __frcp_rn(1.f + __expf(-v[e]));
            uint2 pk = make_uint2(pack2(o[0], o[1]), pack2(o[2], o[3]));
            *(uint2*)(hbuf + (size_t)row * F + col0) = pk;
          } else {
            uint2 pk = make_uint2(pack2(v[0], v[1]), pack2(v[2], v[3]));
            *(uint2*)(qb + (size_t)row * D + (col0 - F)) = pk;
          }
        }
      }
  } else {
    constexpr int PK_PITCH = 608, VT_PITCH = 352;
    char* Pk = smem;
    char* Vt = smem + 128 * PK_PITCH;
    float* Wf = (float*)(smem + 128 * PK_PITCH + 128 * VT_PITCH);
    __syncthreads();
#pragma unroll
    for (int m = 0; m < 4; ++m)
#pragma unroll
      for (int j = 0; j < NTW; ++j) {
        int T = NTW * wc + j;
        if (T < 18) {
          int row = wr * 64 + m * 16 + fr;
#pragma unroll
          for (int e = 0; e < 4; ++e) {
            int c = T * 16 + fq * 4 + e;
            int part = c >= 144 ? 1 : 0, cc = c - 144 * part, hd = cc / 18, d = cc - hd * 18;
            u16 val = f2bf(acc[m][j][e]);
            if (part == 0) *(u16*)(Pk + row * PK_PITCH + (hd * 36 + d) * 2) = val;
            else *(u16*)(Vt + row * VT_PITCH + (hd * 20 + d) * 2) = val;
          }
        }
      }
    for (int i = tid; i < 128 * 8; i += 512) { int row = i >> 3, hd = i & 7; *(u16*)(Vt + row * VT_PITCH + (hd * 20 + 18) * 2) = 0x3f80; }
    for (int i = tid; i < 8 * 324; i += 512) Wf[i] = bf2f(wphi[ct * 8 * 324 + i]);
    __syncthreads();
    {
      const float* W = Wf + wid * 324;
      {
        unsigned* pr0 = (unsigned*)(Pk + lane * PK_PITCH + wid * 72);
        unsigned* pr1 = (unsigned*)(Pk + (lane + 64) * PK_PITCH + wid * 72);
        float k0[18], k1[18];
#pragma unroll
        for (int i = 0; i < 9; ++i) {
          unsigned u = pr0[i]; k0[2 * i] = __uint_as_float(u << 16); k0[2 * i + 1] = __uint_as_float(u & 0xffff0000u);
          unsigned w = pr1[i]; k1[2 * i] = __uint_as_float(w << 16); k1[2 * i + 1] = __uint_as_float(w & 0xffff0000u);
        }
        float p0[18], p1[18];
#pragma unroll
        for (int f = 0; f < 18; ++f) { p0[f] = 0.f; p1[f] = 0.f; }
#pragma unroll
        for (int d = 0; d < 18; ++d)
#pragma unroll
          for (int f = 0; f < 18; ++f) { float w = W[d * 18 + f]; p0[f] = fmaf(k0[d], w, p0[f]); p1[f] = fmaf(k1[d], w, p1[f]); }
        auto finish = [&](float* p, unsigned* pr) {
          float mx = -1e30f, mn = 1e30f;
#pragma unroll
          for (int f = 0; f < 18; ++f) { p[f] = bf2f(f2bf(p[f])); mx = fmaxf(mx, p[f]); mn = fminf(mn, p[f]); }
          float e1[18], e2[18], s1 = 0.f, s2 = 0.f;
#pragma unroll
          for (int f = 0; f < 18; ++f) { e1[f] = __expf(p[f] - mx); s1 += e1[f]; e2[f] = __expf(mn - p[f]); s2 += e2[f]; }
          float r1 = 1.f / s1, r2 = 1.f / s2;
#pragma unroll
          for (int i = 0; i < 9; ++i) { pr[i] = pack2(e1[2 * i] * r1, e1[2 * i + 1] * r1); pr[9 + i] = pack2(e2[2 * i] * r2, e2[2 * i + 1] * r2); }
        };
        finish(p0, pr0);
        finish(p1, pr1);
      }
    }
    __syncthreads();
    {
      typedef LDS_AS s16x4* lp;
      f32x4 kvacc[3][2];
#pragma unroll
      for (int a = 0; a < 3; ++a) for (int b = 0; b < 2; ++b) kvacc[a][b] = f32x4{0.f, 0.f, 0.f, 0.f};
      const int q = fr >> 2, p = lane & 3;
#pragma unroll
      for (int ks = 0; ks < 4; ++ks) {
        bf16x8 pa[3], vb[2];
        int r0 = ks * 32 + 8 * fq + q;
#pragma unroll
        for (int mt = 0; mt < 3; ++mt) {
          const char* a0 = Pk + r0 * PK_PITCH + (wid * 36 + mt * 16 + 4 * p) * 2;
          s16x4 lo = __builtin_amdgcn_ds_read_tr16_b64_v4i16((lp)(a0));
          s16x4 hi = __builtin_amdgcn_ds_read_tr16_b64_v4i16((lp)(a0 + 4 * PK_PITCH));
          pa[mt] = __builtin_shufflevector(lo, hi, 0, 1, 2, 3, 4, 5, 6, 7);
        }
#pragma unroll
        for (int nt = 0; nt < 2; ++nt) {
          const char* b0 = Vt + r0 * VT_PITCH + (wid * 20 + nt * 16 + 4 * p) * 2;
          s16x4 lo = __builtin_amdgcn_ds_read_tr16_b64_v4i16((lp)(b0));
          s16x4 hi = __builtin_amdgcn_ds_read_tr16_b64_v4i16((lp)(b0 + 4 * VT_PITCH));
          vb[nt] = __builtin_shufflevector(lo, hi, 0, 1, 2, 3, 4, 5, 6, 7);
        }
#pragma unroll
        for (int mt = 0; mt < 3; ++mt)
#pragma unroll
          for (int nt = 0; nt < 2; ++nt) kvacc[mt][nt] = __builtin_amdgcn_mfma_f32_16x16x32_bf16(pa[mt], vb[nt], kvacc[mt][nt], 0, 0, 0);
      }
      float* dst = kvslab + ((size_t)rt * 32 + ct * 8 + wid) * 720;
#pragma unroll
      for (int mt = 0; mt < 3; ++mt)
#pragma unroll
        for (int nt = 0; nt < 2; ++nt)
#pragma unroll
          for (int e = 0; e < 4; ++e) {
            int f = mt * 16 + fq * 4 + e, d = nt * 16 + fr;
            if (f < 36 && d < 19) dst[f * 20 + d] = kvacc[mt][nt][e];
          }
    }
  }
}

__global__ __launch_bounds__(512, 2) void k_l1(const u16* __restrict__ xn, const u16* __restrict__ w1, const u16* __restrict__ wqkv,
                                               const u16* __restrict__ wphi, u16* __restrict__ hbuf, u16* __restrict__ qb, float* __restrict__ kvslab) {
  __shared__ __attribute__((aligned(16))) char smem[163840];
  int bid = blockIdx.x, xcd = bid & 7, idx = bid >> 3;
  int rt = (xcd & 1) * 8 + (idx & 7);
  int cs = (xcd >> 1) * 4 + (idx >> 3);
  if (cs < 12) l1_wg<0>(smem, xn, w1, wqkv, wphi, hbuf, qb, kvslab, rt, cs);
  else l1_wg<1>(smem, xn, w1, wqkv, wphi, hbuf, qb, kvslab, rt, cs - 12);
}

__global__ __launch_bounds__(256) void k_kvred(const float* __restrict__ slab, float* __restrict__ kv) {
  int i = blockIdx.x * 256 + threadIdx.x; if (i >= 32 * 720) return;
  float s = 0.f; for (int r = 0; r < 16; ++r) s += slab[(size_t)r * 32 * 720 + i];
  kv[i] = bf2f(f2bf(s));
}
__global__ __launch_bounds__(256) void k_qo(const u16* __restrict__ qb, const u16* __restrict__ wphi, const float* __restrict__ kv, u16* __restrict__ o) {
  int idx = blockIdx.x * 256 + threadIdx.x;
  int h = idx & 31, row = idx >> 5;
  const u16* src = qb + (size_t)row * D + h * DH;
  float xq[DH]; for (int d = 0; d < DH; ++d) xq[d] = bf2f(src[d]);
  float p[DH];
  for (int f = 0; f < DH; ++f) { float s = 0.f; for (int d = 0; d < DH; ++d) s += xq[d] * bf2f(wphi[(h * DH + d) * DH + f]); p[f] = bf2f(f2bf(s)); }
  float mx = p[0], mn = p[0];
  for (int f = 1; f < DH; ++f) { mx = fmaxf(mx, p[f]); mn = fminf(mn, p[f]); }
  float e1[DH], e2[DH], s1 = 0.f, s2 = 0.f;
  for (int f = 0; f < DH; ++f) { e1[f] = __expf(p[f] - mx); s1 += e1[f]; e2[f] = __expf(mn - p[f]); s2 += e2[f]; }
  float phi[36];
  for (int f = 0; f < DH; ++f) { phi[f] = bf2f(f2bf(e1[f] / s1)); phi[18 + f] = bf2f(f2bf(e2[f] / s2)); }
  float num[19]; for (int d = 0; d < 19; ++d) num[d] = 0.f;
  for (int f = 0; f < 36; ++f) for (int d = 0; d < 19; ++d) num[d] += phi[f] * kv[(h * 36 + f) * 20 + d];
  float den = bf2f(f2bf(num[18])) + 1e-6f;
  for (int d = 0; d < 18; ++d) o[(size_t)row * D + h * DH + d] = f2bf(bf2f(f2bf(num[d])) / den);
}
template <int EPI>
__global__ __launch_bounds__(256) void k_gemm(const u16* __restrict__ A, int lda, const u16* __restrict__ B, int ldb,
                                              u16* __restrict__ C, float* __restrict__ Cf, int ldc, int K) {
  __shared__ u16 sA[64][40];
  __shared__ u16 sB[64][40];
  int bm = blockIdx.y * 64, bn = blockIdx.x * 64;
  int tid = threadIdx.x, wid = tid >> 6, lane = tid & 63, fr = lane & 15, fq = lane >> 4;
  int wr = wid >> 1, wc = wid & 1;
  f32x4 acc[2][2] = {};
  for (int k0 = 0; k0 < K; k0 += 32) {
    for (int e = tid; e < 64 * 32; e += 256) {
      int r = e >> 5, c = e & 31;
      sA[r][c] = A[(size_t)(bm + r) * lda + k0 + c];
      int kk = e >> 6, n = e & 63;
      sB[n][kk] = B[(size_t)(k0 + kk) * ldb + bn + n];
    }
    __syncthreads();
    bf16x8 a[2], b[2];
    for (int m = 0; m < 2; ++m) a[m] = *(const bf16x8*)&sA[wr * 32 + m * 16 + fr][fq * 8];
    for (int n = 0; n < 2; ++n) b[n] = *(const bf16x8*)&sB[wc * 32 + n * 16 + fr][fq * 8];
    for (int m = 0; m < 2; ++m) for (int n = 0; n < 2; ++n)
      acc[m][n] = __builtin_amdgcn_mfma_f32_16x16x32_bf16(a[m], b[n], acc[m][n], 0, 0, 0);
    __syncthreads();
  }
  for (int m = 0; m < 2; ++m) for (int n = 0; n < 2; ++n) for (int j = 0; j < 4; ++j) {
    int r = bm + wr * 32 + m * 16 + fq * 4 + j, c = bn + wc * 32 + n * 16 + fr;
    Cf[(size_t)r * ldc + c] = acc[m][n][j];
  }
}
__global__ __launch_bounds__(256) void k_final(const u16* __restrict__ x, const float* __restrict__ a, const float* __restrict__ b, u16* __restrict__ out, int n) {
  int i = blockIdx.x * 256 + threadIdx.x; if (i >= n) return;
  out[i] = f2bf(bf2f(x[i]) + a[i] + b[i]);
}

extern "C" void kernel_launch(void* const* d_in, const int* in_sizes, int n_in, void* d_out, int out_size, void* d_ws, size_t ws_size, hipStream_t stream) {
  const u16* x = (const u16*)d_in[0]; const u16* wqkv = (const u16*)d_in[1]; const u16* wphi = (const u16*)d_in[2]; const u16* wo = (const u16*)d_in[3];
  const u16* w1 = (const u16*)d_in[5]; const u16* w2 = (const u16*)d_in[6];
  u16* out = (u16*)d_out;
  char* ws = (char*)d_ws;
  u16* xn = (u16*)ws; ws += (size_t)S * D * 2;
  u16* qb = (u16*)ws; ws += (size_t)S * D * 2;
  u16* hbuf = (u16*)ws; ws += (size_t)S * F * 2;
  float* kvslab = (float*)ws; ws += 16 * 32 * 720 * 4;
  float* kv = (float*)ws; ws += 32 * 720 * 4;
  u16* o = (u16*)ws; ws += (size_t)S * D * 2;
  float* oa = (float*)ws; ws += (size_t)S * D * 4;
  float* ob = (float*)ws; ws += (size_t)S * D * 4;
  k_ln<<<S / 4, 256, 0, stream>>>(x, xn);
  k_l1<<<256, 512, 0, stream>>>(xn, w1, wqkv, wphi, hbuf, qb, kvslab);
  k_kvred<<<90, 256, 0, stream>>>(kvslab, kv);
  k_qo<<<S * H / 256, 256, 0, stream>>>(qb, wphi, kv, o);
  k_gemm<2><<<dim3(D / 64, S / 64), 256, 0, stream>>>(o, D, wo, D, nullptr, oa, D, D);
  k_gemm<2><<<dim3(D / 64, S / 64), 256, 0, stream>>>(hbuf, F, w2, D, nullptr, ob, D, F);
  k_final<<<(S * D + 255) / 256, 256, 0, stream>>>(x, oa, ob, out, S * D);
}
```

```cpp
#include <hip/hip_runtime.h>
#include <hip/hip_bf16.h>
#include <stdint.h>

typedef __attribute__((ext_vector_type(8))) short bf16x8;
typedef __attribute__((ext_vector_type(4))) short s16x4;
typedef __attribute__((ext_vector_type(4))) float f32x4;
typedef unsigned short u16;
#define LDS_AS __attribute__((address_space(3)))

__device__ __forceinline__ float bf2f(u16 v) { return __uint_as_float(((unsigned)v) << 16); }
__device__ __forceinline__ u16 f2bf(float f) {
  unsigned u = __float_as_uint(f);
  u += 0x7fffu + ((u >> 16) & 1u);
  return (u16)(u >> 16);
}
__device__ __forceinline__ unsigned pack2(float lo, float hi) { return (unsigned)f2bf(lo) | ((unsigned)f2bf(hi) << 16); }

constexpr int S = 2048, D = 576, H = 32, DH = 18, F = 4608;

__global__ __launch_bounds__(256) void k_ln(const u16* __restrict__ x, u16* __restrict__ xn) {
  int row = blockIdx.x * 4 + (threadIdx.x >> 6), lane = threadIdx.x & 63;
  const u16* xr = x + (size_t)row * D;
  float v[9]; float s = 0.f;
  for (int i = 0; i < 9; ++i) { v[i] = bf2f(xr[lane + 64 * i]); s += v[i]; }
  for (int o = 32; o; o >>= 1) s += __shfl_xor(s, o);
  float m = s * (1.f / D);
  float q = 0.f;
  for (int i = 0; i < 9; ++i) { float d = v[i] - m; q += d * d; }
  for (int o = 32; o; o >>= 1) q += __shfl_xor(q, o);
  float r = rsqrtf(q * (1.f / D) + 1e-5f);
  for (int i = 0; i < 9; ++i) xn[(size_t)row * D + lane + 64 * i] = f2bf((v[i] - m) * r);
}

#define GLDS(g, l) __builtin_amdgcn_global_load_lds((const unsigned*)(g), (unsigned*)(l), 16, 0, 0)
#define WAIT_V(n) asm volatile("s_waitcnt vmcnt(" #n ")" ::: "memory")
#define WAIT_L0() asm volatile("s_waitcnt lgkmcnt(0)" ::: "memory")
#define BAR() __builtin_amdgcn_s_barrier()

constexpr int L1_STAGE = 40960;

__device__ __forceinline__ bf16x8 tr_frag(const char* p) {
  typedef LDS_AS s16x4* lp;
  s16x4 lo = __builtin_amdgcn_ds_read_tr16_b64_v4i16((lp)(p));
  s16x4 hi = __builtin_amdgcn_ds_read_tr16_b64_v4i16((lp)(p + 4096));
  return __builtin_shufflevector(lo, hi, 0, 1, 2, 3, 4, 5, 6, 7);
}

template <int TYPE>
__device__ __forceinline__ void l1_wg(char* smem, const u16* __restrict__ xn, const u16* __restrict__ w1, const u16* __restrict__ wqkv,
                                      const u16* __restrict__ wphi, u16* __restrict__ hbuf, u16* __restrict__ qb, float* __restrict__ kvslab,
                                      int rt, int ct) {
  constexpr int NCH = TYPE ? 36 : 54;
  constexpr int NTW = TYPE ? 5 : 7;
  const int tid = threadIdx.x, lane = tid & 63;
  const int wid = __builtin_amdgcn_readfirstlane(tid >> 6);
  const int wr = wid >> 2, wc = wid & 3, fr = lane & 15, fq = lane >> 4;
  const u16* ag = xn + (size_t)(rt * 128 + wid * 16 + (lane >> 2)) * D + (((lane & 3) ^ ((-(lane >> 4)) & 3)) * 8);
  const u16* bg[4]; int bst[4]; bool bval[4];
#pragma unroll
  for (int i = 0; i < 4; ++i) {
    int r = wid * 4 + i;
    int f = 2 * (i | (((wid >> 1) & 1) << 2));
    int lc = lane ^ f;
    bval[i] = lc < NCH;
    int lcc = bval[i] ? lc : 0;
    if (TYPE == 0) {
      int gc = ct * 432 + lcc * 8;
      if (gc < F) { bg[i] = w1 + (size_t)r * F + gc; bst[i] = 32 * F; }
      else { bg[i] = wqkv + (size_t)r * (3 * D) + (gc - F); bst[i] = 32 * 3 * D; }
    } else {
      int part = lcc >= 18 ? 1 : 0, cc = lcc - 18 * part;
      bg[i] = wqkv + (size_t)r * (3 * D) + D + part * D + ct * 144 + cc * 8; bst[i] = 32 * 3 * D;
    }
  }
  auto issue = [&](int sb_off) {
    char* sb = smem + sb_off;
    GLDS(ag, sb + wid * 1024);
    ag += 32;
#pragma unroll
    for (int i = 0; i < 4; ++i) {
      if (bval[i]) GLDS(bg[i], sb + 8192 + (wid * 4 + i) * 1024);
      bg[i] += bst[i];
    }
  };
  const int a_base = (wr * 64 + fr) * 64 + ((fq ^ ((-(fr >> 2)) & 3)) * 16);
  int b_off[NTW];
#pragma unroll
  for (int j = 0; j < NTW; ++j) {
    int c = 2 * (NTW * wc + j) + ((lane & 3) >> 1);
    int f = 2 * ((fr >> 2) | ((fq & 1) << 2));
    b_off[j] = 8192 + (8 * fq + (fr >> 2)) * 1024 + ((c ^ f) * 16) + 8 * (lane & 1);
  }
  f32x4 acc[4][NTW];
#pragma unroll
  for (int m = 0; m < 4; ++m)
#pragma unroll
    for (int j = 0; j < NTW; ++j) acc[m][j] = f32x4{0.f, 0.f, 0.f, 0.f};
  bf16x8 af[4], bf[NTW];
  issue(0); issue(L1_STAGE); issue(2 * L1_STAGE); issue(3 * L1_STAGE);
  WAIT_V(15); BAR();
#pragma unroll
  for (int m = 0; m < 4; ++m) af[m] = *(const bf16x8*)(smem + a_base + m * 1024);
#pragma unroll
  for (int j = 0; j < NTW; ++j) bf[j] = tr_frag(smem + b_off[j]);
#pragma unroll 1
  for (int t = 0; t < 17; ++t) {
    if (t < 15) WAIT_V(10); else if (t == 15) WAIT_V(5); else WAIT_V(0);
    WAIT_L0();
    BAR();
    if (t + 4 < 18) issue((t & 3) * L1_STAGE);
    const char* nb = smem + ((t + 1) & 3) * L1_STAGE;
#pragma unroll
    for (int j = 0; j < NTW; ++j) {
#pragma unroll
      for (int m = 0; m < 4; ++m)
        acc[m][j] = __builtin_amdgcn_mfma_f32_16x16x32_bf16(bf[j], af[m], acc[m][j], 0, 0, 0);
      bf[j] = tr_frag(nb + b_off[j]);
    }
#pragma unroll
    for (int m = 0; m < 4; ++m) af[m] = *(const bf16x8*)(nb + a_base + m * 1024);
  }
#pragma unroll
  for (int j = 0; j < NTW; ++j)
#pragma unroll
    for (int m = 0; m < 4; ++m)
      acc[m][j] = __builtin_amdgcn_mfma_f32_16x16x32_bf16(bf[j], af[m], acc[m][j], 0, 0, 0);
  if (TYPE == 0) {
#pragma unroll
    for (int m = 0; m < 4; ++m)
#pragma unroll
      for (int j = 0; j < NTW; ++j) {
        int T = NTW * wc + j;
        if (T < 27) {
          int row = rt * 128 + wr * 64 + m * 16 + fr;
          int col0 = ct * 432 + T * 16 + fq * 4;
          f32x4 v = acc[m][j];
          if (col0 < F) {
            float o[4];
#pragma unroll
            for (int e = 0; e < 4; ++e) o[e] = v[e] * __frcp_rn(1.f + __expf(-v[e]));
            uint2 pk = make_uint2(pack2(o[0], o[1]), pack2(o[2], o[3]));
            *(uint2*)(hbuf + (size_t)row * F + col0) = pk;
          } else {
            uint2 pk = make_uint2(pack2(v[0], v[1]), pack2(v[2], v[3]));
            *(uint2*)(qb + (size_t)row * D + (col0 - F)) = pk;
          }
        }
      }
  } else {
    constexpr int PK_PITCH = 608, VT_PITCH = 352;
    char* Pk = smem;
    char* Vt = smem + 128 * PK_PITCH;
    float* Wf = (float*)(smem + 128 * PK_PITCH + 128 * VT_PITCH);
    __syncthreads();
#pragma unroll
    for (int m = 0; m < 4; ++m)
#pragma unroll
      for (int j = 0; j < NTW; ++j) {
        int T = NTW * wc + j;
        if (T < 18) {
          int row = wr * 64 + m * 16 + fr;
#pragma unroll
          for (int e = 0; e < 4; ++e) {
            int c = T * 16 + fq * 4 + e;
            int part = c >= 144 ? 1 : 0, cc = c - 144 * part, hd = cc / 18, d = cc - hd * 18;
            u16 val = f2bf(acc[m][j][e]);
            if (part == 0) *(u16*)(Pk + row * PK_PITCH + (hd * 36 + d) * 2) = val;
            else *(u16*)(Vt + row * VT_PITCH + (hd * 20 + d) * 2) = val;
          }
        }
      }
    for (int i = tid; i < 128 * 8; i += 512) { int row = i >> 3, hd = i & 7; *(u16*)(Vt + row * VT_PITCH + (hd * 20 + 18) * 2) = 0x3f80; }
    for (int i = tid; i < 8 * 324; i += 512) Wf[i] = bf2f(wphi[ct * 8 * 324 + i]);
    __syncthreads();
    {
      const float* W = Wf + wid * 324;
      {
        unsigned* pr0 = (unsigned*)(Pk + lane * PK_PITCH + wid * 72);
        unsigned* pr1 = (unsigned*)(Pk + (lane + 64) * PK_PITCH + wid * 72);
        float k0[18], k1[18];
#pragma unroll
        for (int i = 0; i < 9; ++i) {
          unsigned u = pr0[i]; k0[2 * i] = __uint_as_float(u << 16); k0[2 * i + 1] = __uint_as_float(u & 0xffff0000u);
          unsigned w = pr1[i]; k1[2 * i] = __uint_as_float(w << 16); k1[2 * i + 1] = __uint_as_float(w & 0xffff0000u);
        }
        float p0[18], p1[18];
#pragma unroll
        for (int f = 0; f < 18; ++f) { p0[f] = 0.f; p1[f] = 0.f; }
#pragma unroll
        for (int d = 0; d < 18; ++d)
#pragma unroll
          for (int f = 0; f < 18; ++f) { float w = W[d * 18 + f]; p0[f] = fmaf(k0[d], w, p0[f]); p1[f] = fmaf(k1[d], w, p1[f]); }
        auto finish = [&](float* p, unsigned* pr) {
          float mx = -1e30f, mn = 1e30f;
#pragma unroll
          for (int f = 0; f < 18; ++f) { p[f] = bf2f(f2bf(p[f])); mx = fmaxf(mx, p[f]); mn = fminf(mn, p[f]); }
          float e1[18], e2[18], s1 = 0.f, s2 = 0.f;
#pragma unroll
          for (int f = 0; f < 18; ++f) { e1[f] = __expf(p[f] - mx); s1 += e1[f]; e2[f] = __expf(mn - p[f]); s2 += e2[f]; }
          float r1 = 1.f / s1, r2 = 1.f / s2;
#pragma unroll
          for (int i = 0; i < 9; ++i) { pr[i] = pack2(e1[2 * i] * r1, e1[2 * i + 1] * r1); pr[9 + i] = pack2(e2[2 * i] * r2, e2[2 * i + 1] * r2); }
        };
        finish(p0, pr0);
        finish(p1, pr1);
      }
    }
    __syncthreads();
    {
      typedef LDS_AS s16x4* lp;
      f32x4 kvacc[3][2];
#pragma unroll
      for (int a = 0; a < 3; ++a) for (int b = 0; b < 2; ++b) kvacc[a][b] = f32x4{0.f, 0.f, 0.f, 0.f};
      const int q = fr >> 2, p = lane & 3;
#pragma unroll
      for (int ks = 0; ks < 4; ++ks) {
        bf16x8 pa[3], vb[2];
        int r0 = ks * 32 + 8 * fq + q;
#pragma unroll
        for (int mt = 0; mt < 3; ++mt) {
          const char* a0 = Pk + r0 * PK_PITCH + (wid * 36 + mt * 16 + 4 * p) * 2;
          s16x4 lo = __builtin_amdgcn_ds_read_tr16_b64_v4i16((lp)(a0));
          s16x4 hi = __builtin_amdgcn_ds_read_tr16_b64_v4i16((lp)(a0 + 4 * PK_PITCH));
          pa[mt] = __builtin_shufflevector(lo, hi, 0, 1, 2, 3, 4, 5, 6, 7);
        }
#pragma unroll
        for (int nt = 0; nt < 2; ++nt) {
          const char* b0 = Vt + r0 * VT_PITCH + (wid * 20 + nt * 16 + 4 * p) * 2;
          s16x4 lo = __builtin_amdgcn_ds_read_tr16_b64_v4i16((lp)(b0));
          s16x4 hi = __builtin_amdgcn_ds_read_tr16_b64_v4i16((lp)(b0 + 4 * VT_PITCH));
          vb[nt] = __builtin_shufflevector(lo, hi, 0, 1, 2, 3, 4, 5, 6, 7);
        }
#pragma unroll
        for (int mt = 0; mt < 3; ++mt)
#pragma unroll
          for (int nt = 0; nt < 2; ++nt) kvacc[mt][nt] = __builtin_amdgcn_mfma_f32_16x16x32_bf16(pa[mt], vb[nt], kvacc[mt][nt], 0, 0, 0);
      }
      float* dst = kvslab + ((size_t)rt * 32 + ct * 8 + wid) * 720;
#pragma unroll
      for (int mt = 0; mt < 3; ++mt)
#pragma unroll
        for (int nt = 0; nt < 2; ++nt)
#pragma unroll
          for (int e = 0; e < 4; ++e) {
            int f = mt * 16 + fq * 4 + e, d = nt * 16 + fr;
            if (f < 36 && d < 19) dst[f * 20 + d] = kvacc[mt][nt][e];
          }
    }
  }
}

__global__ __launch_bounds__(512, 2) void k_l1(const u16* __restrict__ xn, const u16* __restrict__ w1, const u16* __restrict__ wqkv,
                                               const u16* __restrict__ wphi, u16* __restrict__ hbuf, u16* __restrict__ qb, float* __restrict__ kvslab) {
  __shared__ __attribute__((aligned(16))) char smem[163840];
  int bid = blockIdx.x, xcd = bid & 7, idx = bid >> 3;
  int rt = (xcd & 1) * 8 + (idx & 7);
  int cs = (xcd >> 1) * 4 + (idx >> 3);
  if (cs < 12) l1_wg<0>(smem, xn, w1, wqkv, wphi, hbuf, qb, kvslab, rt, cs);
  else l1_wg<1>(smem, xn, w1, wqkv, wphi, hbuf, qb, kvslab, rt, cs - 12);
}


constexpr int G2_STAGE = 17408, G2_NS = 8;
__device__ __forceinline__ int swap23(int r) { return (r & ~0xC) | ((r & 4) << 1) | ((r & 8) >> 1); }

template <int NIT>
__device__ __forceinline__ void g2_main(char* smem, const u16* __restrict__ A, int lda, const u16* __restrict__ B, int ldb,
                                        f32x4 (&acc)[2][5]) {
  const int tid = threadIdx.x, lane = tid & 63;
  const int wid = __builtin_amdgcn_readfirstlane(tid >> 6);
  const int wr = wid >> 1, wc = wid & 1, fr = lane & 15, fq = lane >> 4;
  const u16* ag = A + (size_t)(wid * 16 + (lane >> 2)) * lda + (((lane & 3) ^ ((-(lane >> 4)) & 3)) * 8);
  const u16* bg0; const u16* bg1;
  { int ci = wid * 64 + lane; int rho = ci / 18, c = ci - rho * 18; bg0 = B + (size_t)swap23(rho) * ldb + c * 8; }
  { int ci = 8 * 64 + lane; int rho = ci / 18, c = ci - rho * 18; bg1 = B + (size_t)swap23(rho) * ldb + c * 8; }
  const size_t bstep = (size_t)32 * ldb;
  auto issue = [&](int sb_off) {
    char* sb = smem + sb_off;
    GLDS(ag, sb + wid * 1024); ag += 32;
    GLDS(bg0, sb + 8192 + wid * 1024); bg0 += bstep;
    if (wid == 0) { GLDS(bg1, sb + 8192 + 8192); } bg1 += bstep;
  };
  const int a_base = (wr * 32 + fr) * 64 + ((fq ^ ((-(fr >> 2)) & 3)) * 16);
  const int b_base = 8192 + (16 * (fq >> 1) + 4 * (fq & 1) + (fr >> 2)) * 288 + (2 * (5 * wc) + ((lane & 3) >> 1)) * 16 + 8 * (lane & 1);
  bf16x8 af[2], bf[5];
  auto ldb_frag = [&](const char* sb, int j) -> bf16x8 {
    typedef LDS_AS s16x4* lp;
    s16x4 lo = __builtin_amdgcn_ds_read_tr16_b64_v4i16((lp)(sb + b_base + j * 32));
    s16x4 hi = __builtin_amdgcn_ds_read_tr16_b64_v4i16((lp)(sb + b_base + j * 32 + 8 * 288));
    return __builtin_shufflevector(lo, hi, 0, 1, 2, 3, 4, 5, 6, 7);
  };
#pragma unroll
  for (int s = 0; s < G2_NS; ++s) issue(s * G2_STAGE);
  if (wid == 0) WAIT_V(21); else WAIT_V(14);
  BAR();
#pragma unroll
  for (int m = 0; m < 2; ++m) af[m] = *(const bf16x8*)(smem + a_base + m * 1024);
#pragma unroll
  for (int j = 0; j < 5; ++j) bf[j] = ldb_frag(smem, j);
  auto step = [&](int t, bool pre) {
    const char* nb = smem + ((t + 1) & (G2_NS - 1)) * G2_STAGE;
#pragma unroll
    for (int j = 0; j < 5; ++j) {
#pragma unroll
      for (int m = 0; m < 2; ++m)
        acc[m][j] = __builtin_amdgcn_mfma_f32_16x16x32_bf16(bf[j], af[m], acc[m][j], 0, 0, 0);
      if (pre) bf[j] = ldb_frag(nb, j);
    }
    if (pre) {
#pragma unroll
      for (int m = 0; m < 2; ++m) af[m] = *(const bf16x8*)(nb + a_base + m * 1024);
    }
  };
#pragma unroll 1
  for (int t = 0; t < NIT - G2_NS; ++t) {
    if (wid == 0) WAIT_V(18); else WAIT_V(12);
    WAIT_L0(); BAR();
    issue((t & (G2_NS - 1)) * G2_STAGE);
    step(t, true);
  }
#define G2_TAIL(R, N3, N2) { if (wid == 0) WAIT_V(N3); else WAIT_V(N2); WAIT_L0(); BAR(); step(NIT - G2_NS + R, true); }
  G2_TAIL(0, 18, 12) G2_TAIL(1, 15, 10) G2_TAIL(2, 12, 8) G2_TAIL(3, 9, 6) G2_TAIL(4, 6, 4) G2_TAIL(5, 3, 2) G2_TAIL(6, 0, 0)
  WAIT_L0();
  step(NIT - 1, false);
}

__global__ __launch_bounds__(512, 2) void k_g2(const u16* __restrict__ hbuf, const u16* __restrict__ w2, float* __restrict__ slab) {
  __shared__ __attribute__((aligned(16))) char smem[G2_NS * G2_STAGE + 64];
  int bid = blockIdx.x, xcd = bid & 7, idx = bid >> 3;
  int rt = xcd * 2 + (idx & 1), ct = (idx >> 1) & 3, ks = idx >> 3;
  f32x4 acc[2][5];
#pragma unroll
  for (int m = 0; m < 2; ++m)
#pragma unroll
    for (int j = 0; j < 5; ++j) acc[m][j] = f32x4{0.f, 0.f, 0.f, 0.f};
  g2_main<36>(smem, hbuf + (size_t)rt * 128 * F + ks * 1152, F, w2 + (size_t)ks * 1152 * D + ct * 144, D, acc);
  const int tid = threadIdx.x, lane = tid & 63, wid = tid >> 6, wr = wid >> 1, wc = wid & 1, fr = lane & 15, fq = lane >> 4;
  float* dst = slab + (size_t)ks * S * D;
#pragma unroll
  for (int m = 0; m < 2; ++m)
#pragma unroll
    for (int j = 0; j < 5; ++j) {
      int T = 5 * wc + j;
      if (T < 9) *(f32x4*)(dst + (size_t)(rt * 128 + wr * 32 + m * 16 + fr) * D + ct * 144 + T * 16 + fq * 4) = acc[m][j];
    }
}

__global__ __launch_bounds__(512, 2) void k_l3(const u16* __restrict__ o, const u16* __restrict__ wo, const float* __restrict__ slab,
                                               const u16* __restrict__ x, u16* __restrict__ out) {
  __shared__ __attribute__((aligned(16))) char smem[G2_NS * G2_STAGE + 64];
  int bid = blockIdx.x; int rt = bid >> 2, ct = bid & 3;
  f32x4 acc[2][5];
#pragma unroll
  for (int m = 0; m < 2; ++m)
#pragma unroll
    for (int j = 0; j < 5; ++j) acc[m][j] = f32x4{0.f, 0.f, 0.f, 0.f};
  g2_main<18>(smem, o + (size_t)rt * 128 * D, D, wo + ct * 144, D, acc);
  const int tid = threadIdx.x, lane = tid & 63, wid = tid >> 6, wr = wid >> 1, wc = wid & 1, fr = lane & 15, fq = lane >> 4;
#pragma unroll
  for (int m = 0; m < 2; ++m)
#pragma unroll
    for (int j = 0; j < 5; ++j) {
      int T = 5 * wc + j;
      if (T < 9) {
        size_t off = (size_t)(rt * 128 + wr * 32 + m * 16 + fr) * D + ct * 144 + T * 16 + fq * 4;
        f32x4 v = acc[m][j];
#pragma unroll
        for (int k = 0; k < 4; ++k) v += *(const f32x4*)(slab + (size_t)k * S * D + off);
        uint2 xv = *(const uint2*)(x + off);
        float x0 = __uint_as_float(xv.x << 16), x1 = __uint_as_float(xv.x & 0xffff0000u), x2 = __uint_as_float(xv.y << 16), x3 = __uint_as_float(xv.y & 0xffff0000u);
        *(uint2*)(out + off) = make_uint2(pack2(v[0] + x0, v[1] + x1), pack2(v[2] + x2, v[3] + x3));
      }
    }
}

__global__ __launch_bounds__(256) void k_kvred(const float* __restrict__ slab, float* __restrict__ kv) {
  int i = blockIdx.x * 256 + threadIdx.x; if (i >= 32 * 720) return;
  float s = 0.f; for (int r = 0; r < 16; ++r) s += slab[(size_t)r * 32 * 720 + i];
  kv[i] = bf2f(f2bf(s));
}
__global__ __launch_bounds__(256) void k_qo(const u16* __restrict__ qb, const u16* __restrict__ wphi, const float* __restrict__ kv, u16* __restrict__ o) {
  int idx = blockIdx.x * 256 + threadIdx.x;
  int h = idx & 31, row = idx >> 5;
  const u16* src = qb + (size_t)row * D + h * DH;
  float xq[DH]; for (int d = 0; d < DH; ++d) xq[d] = bf2f(src[d]);
  float p[DH];
  for (int f = 0; f < DH; ++f) { float s = 0.f; for (int d = 0; d < DH; ++d) s += xq[d] * bf2f(wphi[(h * DH + d) * DH + f]); p[f] = bf2f(f2bf(s)); }
  float mx = p[0], mn = p[0];
  for (int f = 1; f < DH; ++f) { mx = fmaxf(mx, p[f]); mn = fminf(mn, p[f]); }
  float e1[DH], e2[DH], s1 = 0.f, s2 = 0.f;
  for (int f = 0; f < DH; ++f) { e1[f] = __expf(p[f] - mx); s1 += e1[f]; e2[f] = __expf(mn - p[f]); s2 += e2[f]; }
  float phi[36];
  for (int f = 0; f < DH; ++f) { phi[f] = bf2f(f2bf(e1[f] / s1)); phi[18 + f] = bf2f(f2bf(e2[f] / s2)); }
  float num[19]; for (int d = 0; d < 19; ++d) num[d] = 0.f;
  for (int f = 0; f < 36; ++f) for (int d = 0; d < 19; ++d) num[d] += phi[f] * kv[(h * 36 + f) * 20 + d];
  float den = bf2f(f2bf(num[18])) + 1e-6f;
  for (int d = 0; d < 18; ++d) o[(size_t)row * D + h * DH + d] = f2bf(bf2f(f2bf(num[d])) / den);
}
template <int EPI>
__global__ __launch_bounds__(256) void k_gemm(const u16* __restrict__ A, int lda, const u16* __restrict__ B, int ldb,
                                              u16* __restrict__ C, float* __restrict__ Cf, int ldc, int K) {
  __shared__ u16 sA[64][40];
  __shared__ u16 sB[64][40];
  int bm = blockIdx.y * 64, bn = blockIdx.x * 64;
  int tid = threadIdx.x, wid = tid >> 6, lane = tid & 63, fr = lane & 15, fq = lane >> 4;
  int wr = wid >> 1, wc = wid & 1;
  f32x4 acc[2][2] = {};
  for (int k0 = 0; k0 < K; k0 += 32) {
    for (int e = tid; e < 64 * 32; e += 256) {
      int r = e >> 5, c = e & 31;
      sA[r][c] = A[(size_t)(bm + r) * lda + k0 + c];
      int kk = e >> 6, n = e & 63;
      sB[n][kk] = B[(size_t)(k0 + kk) * ldb + bn + n];
    }
    __syncthreads();
    bf16x8 a[2], b[2];
    for (int m = 0; m < 2; ++m) a[m] = *(const bf16x8*)&sA[wr * 32 + m * 16 + fr][fq * 8];
    for (int n = 0; n < 2; ++n) b[n] = *(const bf16x8*)&sB[wc * 32 + n * 16 + fr][fq * 8];
    for (int m = 0; m < 2; ++m) for (int n = 0; n < 2; ++n)
      acc[m][n] = __builtin_amdgcn_mfma_f32_16x16x32_bf16(a[m], b[n], acc[m][n], 0, 0, 0);
    __syncthreads();
  }
  for (int m = 0; m < 2; ++m) for (int n = 0; n < 2; ++n) for (int j = 0; j < 4; ++j) {
    int r = bm + wr * 32 + m * 16 + fq * 4 + j, c = bn + wc * 32 + n * 16 + fr;
    Cf[(size_t)r * ldc + c] = acc[m][n][j];
  }
}
__global__ __launch_bounds__(256) void k_final(const u16* __restrict__ x, const float* __restrict__ a, const float* __restrict__ b, u16* __restrict__ out, int n) {
  int i = blockIdx.x * 256 + threadIdx.x; if (i >= n) return;
  out[i] = f2bf(bf2f(x[i]) + a[i] + b[i]);
}

extern "C" void kernel_launch(void* const* d_in, const int* in_sizes, int n_in, void* d_out, int out_size, void* d_ws, size_t ws_size, hipStream_t stream) {
  const u16* x = (const u16*)d_in[0]; const u16* wqkv = (const u16*)d_in[1]; const u16* wphi = (const u16*)d_in[2]; const u16* wo = (const u16*)d_in[3];
  const u16* w1 = (const u16*)d_in[5]; const u16* w2 = (const u16*)d_in[6];
  u16* out = (u16*)d_out;
  char* ws = (char*)d_ws;
  u16* xn = (u16*)ws; ws += (size_t)S * D * 2;
  u16* qb = (u16*)ws; ws += (size_t)S * D * 2;
  u16* hbuf = (u16*)ws; ws += (size_t)S * F * 2;
  float* kvslab = (float*)ws; ws += 16 * 32 * 720 * 4;
  float* kv = (float*)ws; ws += 32 * 720 * 4;
  u16* o = (u16*)ws; ws += (size_t)S * D * 2;
  float* slab = (float*)ws; ws += (size_t)4 * S * D * 4;
  k_ln<<<S / 4, 256, 0, stream>>>(x, xn);
  k_l1<<<256, 512, 0, stream>>>(xn, w1, wqkv, wphi, hbuf, qb, kvslab);
  k_kvred<<<90, 256, 0, stream>>>(kvslab, kv);
  k_qo<<<S * H / 256, 256, 0, stream>>>(qb, wphi, kv, o);
  k_g2<<<256, 512, 0, stream>>>(hbuf, w2, slab);
  k_l3<<<64, 512, 0, stream>>>(o, wo, slab, x, out);
}
```

```cpp
#include <hip/hip_runtime.h>
#include <hip/hip_bf16.h>
#include <stdint.h>

typedef __attribute__((ext_vector_type(8))) short bf16x8;
typedef __attribute__((ext_vector_type(4))) short s16x4;
typedef __attribute__((ext_vector_type(4))) float f32x4;
typedef unsigned short u16;
#define LDS_AS __attribute__((address_space(3)))

__device__ __forceinline__ float bf2f(u16 v) { return __uint_as_float(((unsigned)v) << 16); }
__device__ __forceinline__ u16 f2bf(float f) {
  unsigned u = __float_as_uint(f);
  u += 0x7fffu + ((u >> 16) & 1u);
  return (u16)(u >> 16);
}
__device__ __forceinline__ unsigned pack2(float lo, float hi) { return (unsigned)f2bf(lo) | ((unsigned)f2bf(hi) << 16); }

constexpr int S = 2048, D = 576, H = 32, DH = 18, F = 4608;

#define STAMPK(k, i)
__global__ __launch_bounds__(256) void k_ln(const u16* __restrict__ x, u16* __restrict__ xn) {
  int row = blockIdx.x * 4 + (threadIdx.x >> 6), lane = threadIdx.x & 63;
  const u16* xr = x + (size_t)row * D;
  float v[9]; float s = 0.f;
  for (int i = 0; i < 9; ++i) { v[i] = bf2f(xr[lane + 64 * i]); s += v[i]; }
  for (int o = 32; o; o >>= 1) s += __shfl_xor(s, o);
  float m = s * (1.f / D);
  float q = 0.f;
  for (int i = 0; i < 9; ++i) { float d = v[i] - m; q += d * d; }
  for (int o = 32; o; o >>= 1) q += __shfl_xor(q, o);
  float r = rsqrtf(q * (1.f / D) + 1e-5f);
  for (int i = 0; i < 9; ++i) xn[(size_t)row * D + lane + 64 * i] = f2bf((v[i] - m) * r);
}

#define GLDS(g, l) __builtin_amdgcn_global_load_lds((const unsigned*)(g), (unsigned*)(l), 16, 0, 0)
#define WAIT_V(n) asm volatile("s_waitcnt vmcnt(" #n ")" ::: "memory")
#define WAIT_L0() asm volatile("s_waitcnt lgkmcnt(0)" ::: "memory")
#define BAR() __builtin_amdgcn_s_barrier()

constexpr int L1_STAGE = 40960;
#define SCHED_IL() do { _Pragma("unroll") for (int _i = 0; _i < 9; ++_i) { __builtin_amdgcn_sched_group_barrier(0x100, 2, 0); __builtin_amdgcn_sched_group_barrier(0x8, 1, 0); } __builtin_amdgcn_sched_group_barrier(0x8, 32, 0); } while (0)

__device__ __forceinline__ bf16x8 tr_frag(const char* p) {
  typedef LDS_AS s16x4* lp;
  s16x4 lo = __builtin_amdgcn_ds_read_tr16_b64_v4i16((lp)(p));
  s16x4 hi = __builtin_amdgcn_ds_read_tr16_b64_v4i16((lp)(p + 4096));
  return __builtin_shufflevector(lo, hi, 0, 1, 2, 3, 4, 5, 6, 7);
}

template <int TYPE>
__device__ __forceinline__ void l1_wg(char* smem, const u16* __restrict__ xn, const u16* __restrict__ w1, const u16* __restrict__ wqkv,
                                      const u16* __restrict__ wphi, u16* __restrict__ hbuf, u16* __restrict__ qb, float* __restrict__ kvslab,
                                      int rt, int ct) {
  constexpr int NCH = TYPE ? 36 : 54;
  constexpr int NTW = TYPE ? 5 : 7;
  const int tid = threadIdx.x, lane = tid & 63;
  const int wid = __builtin_amdgcn_readfirstlane(tid >> 6);
  const int wr = wid >> 2, wc = wid & 3, fr = lane & 15, fq = lane >> 4;
  const u16* ag = xn + (size_t)(rt * 128 + wid * 16 + (lane >> 2)) * D + (((lane & 3) ^ ((-(lane >> 4)) & 3)) * 8);
  const u16* bg[4]; int bst[4]; bool bval[4];
#pragma unroll
  for (int i = 0; i < 4; ++i) {
    int r = wid * 4 + i;
    int f = 2 * (i | (((wid >> 1) & 1) << 2));
    int lc = lane ^ f;
    bval[i] = lc < NCH;
    int lcc = bval[i] ? lc : 0;
    if (TYPE == 0) {
      int gc = ct * 432 + lcc * 8;
      if (gc < F) { bg[i] = w1 + (size_t)r * F + gc; bst[i] = 32 * F; }
      else { bg[i] = wqkv + (size_t)r * (3 * D) + (gc - F); bst[i] = 32 * 3 * D; }
    } else {
      int part = lcc >= 18 ? 1 : 0, cc = lcc - 18 * part;
      bg[i] = wqkv + (size_t)r * (3 * D) + D + part * D + ct * 144 + cc * 8; bst[i] = 32 * 3 * D;
    }
  }
  auto issue = [&](int sb_off) {
    char* sb = smem + sb_off;
    GLDS(ag, sb + wid * 1024);
    ag += 32;
#pragma unroll
    for (int i = 0; i < 4; ++i) {
      if (bval[i]) GLDS(bg[i], sb + 8192 + (wid * 4 + i) * 1024);
      bg[i] += bst[i];
    }
  };
  const int a_base = (wr * 64 + fr) * 64 + ((fq ^ ((-(fr >> 2)) & 3)) * 16);
  int b_off[NTW];
#pragma unroll
  for (int j = 0; j < NTW; ++j) {
    int c = 2 * (NTW * wc + j) + ((lane & 3) >> 1);
    int f = 2 * ((fr >> 2) | ((fq & 1) << 2));
    b_off[j] = 8192 + (8 * fq + (fr >> 2)) * 1024 + ((c ^ f) * 16) + 8 * (lane & 1);
  }
  f32x4 acc[4][NTW];
#pragma unroll
  for (int m = 0; m < 4; ++m)
#pragma unroll
    for (int j = 0; j < NTW; ++j) acc[m][j] = f32x4{0.f, 0.f, 0.f, 0.f};
  bf16x8 afA[4], bfA[NTW], afB[4], bfB[NTW];
  auto ldfr = [&](const char* nb, bf16x8 (&af)[4], bf16x8 (&bf)[NTW]) {
#pragma unroll
    for (int j = 0; j < NTW; ++j) bf[j] = tr_frag(nb + b_off[j]);
#pragma unroll
    for (int m = 0; m < 4; ++m) af[m] = *(const bf16x8*)(nb + a_base + m * 1024);
  };
  auto mma = [&](bf16x8 (&af)[4], bf16x8 (&bf)[NTW]) {
#pragma unroll
    for (int j = 0; j < NTW; ++j)
#pragma unroll
      for (int m = 0; m < 4; ++m)
        acc[m][j] = __builtin_amdgcn_mfma_f32_16x16x32_bf16(bf[j], af[m], acc[m][j], 0, 0, 0);
  };
  STAMPK(1, 0);
  issue(0); issue(L1_STAGE); issue(2 * L1_STAGE); issue(3 * L1_STAGE);
  WAIT_V(15); BAR();
  STAMPK(1, 1);
  ldfr(smem, afA, bfA);
#pragma unroll 1
  for (int t = 0; t < 16; t += 2) {
    if (t < 15) WAIT_V(10); else WAIT_V(5);
    WAIT_L0(); BAR();
    if (t + 4 < 18) issue((t & 3) * L1_STAGE);
    ldfr(smem + ((t + 1) & 3) * L1_STAGE, afB, bfB);
    mma(afA, bfA);
    SCHED_IL();
    if (t + 1 < 15) WAIT_V(10); else WAIT_V(5);
    WAIT_L0(); BAR();
    if (t + 5 < 18) issue(((t + 1) & 3) * L1_STAGE);
    ldfr(smem + ((t + 2) & 3) * L1_STAGE, afA, bfA);
    mma(afB, bfB);
    SCHED_IL();
  }
  WAIT_V(0); WAIT_L0(); BAR();
  ldfr(smem + (17 & 3) * L1_STAGE, afB, bfB);
  mma(afA, bfA);
  mma(afB, bfB);
  STAMPK(1, 2);
  if (TYPE == 0) {
#pragma unroll
    for (int m = 0; m < 4; ++m)
#pragma unroll
      for (int j = 0; j < NTW; ++j) {
        int T = NTW * wc + j;
        if (T < 27) {
          int row = rt * 128 + wr * 64 + m * 16 + fr;
          int col0 = ct * 432 + T * 16 + fq * 4;
          f32x4 v = acc[m][j];
          if (col0 < F) {
            float o[4];
#pragma unroll
            for (int e = 0; e < 4; ++e) o[e] = v[e] * __frcp_rn(1.f + __expf(-v[e]));
            uint2 pk = make_uint2(pack2(o[0], o[1]), pack2(o[2], o[3]));
            *(uint2*)(hbuf + (size_t)row * F + col0) = pk;
          } else {
            uint2 pk = make_uint2(pack2(v[0], v[1]), pack2(v[2], v[3]));
            *(uint2*)(qb + (size_t)row * D + (col0 - F)) = pk;
          }
        }
      }
  } else {
    constexpr int PK_PITCH = 608, VT_PITCH = 352;
    char* Pk = smem;
    char* Vt = smem + 128 * PK_PITCH;
    float* Wf = (float*)(smem + 128 * PK_PITCH + 128 * VT_PITCH);
    __syncthreads();
#pragma unroll
    for (int m = 0; m < 4; ++m)
#pragma unroll
      for (int j = 0; j < NTW; ++j) {
        int T = NTW * wc + j;
        if (T < 18) {
          int row = wr * 64 + m * 16 + fr;
#pragma unroll
          for (int e = 0; e < 4; ++e) {
            int c = T * 16 + fq * 4 + e;
            int part = c >= 144 ? 1 : 0, cc = c - 144 * part, hd = cc / 18, d = cc - hd * 18;
            u16 val = f2bf(acc[m][j][e]);
            if (part == 0) *(u16*)(Pk + row * PK_PITCH + (hd * 36 + d) * 2) = val;
            else *(u16*)(Vt + row * VT_PITCH + (hd * 20 + d) * 2) = val;
          }
        }
      }
    for (int i = tid; i < 128 * 8; i += 512) { int row = i >> 3, hd = i & 7; *(u16*)(Vt + row * VT_PITCH + (hd * 20 + 18) * 2) = 0x3f80; }
    for (int i = tid; i < 8 * 324; i += 512) Wf[i] = bf2f(wphi[ct * 8 * 324 + i]);
    __syncthreads();
    {
      const float* W = Wf + wid * 324;
      {
        unsigned* pr0 = (unsigned*)(Pk + lane * PK_PITCH + wid * 72);
        unsigned* pr1 = (unsigned*)(Pk + (lane + 64) * PK_PITCH + wid * 72);
        float k0[18], k1[18];
#pragma unroll
        for (int i = 0; i < 9; ++i) {
          unsigned u = pr0[i]; k0[2 * i] = __uint_as_float(u << 16); k0[2 * i + 1] = __uint_as_float(u & 0xffff0000u);
          unsigned w = pr1[i]; k1[2 * i] = __uint_as_float(w << 16); k1[2 * i + 1] = __uint_as_float(w & 0xffff0000u);
        }
        float p0[18], p1[18];
#pragma unroll
        for (int f = 0; f < 18; ++f) { p0[f] = 0.f; p1[f] = 0.f; }
#pragma unroll
        for (int d = 0; d < 18; ++d)
#pragma unroll
          for (int f = 0; f < 18; ++f) { float w = W[d * 18 + f]; p0[f] = fmaf(k0[d], w, p0[f]); p1[f] = fmaf(k1[d], w, p1[f]); }
        auto finish = [&](float* p, unsigned* pr) {
          float mx = -1e30f, mn = 1e30f;
#pragma unroll
          for (int f = 0; f < 18; ++f) { p[f] = bf2f(f2bf(p[f])); mx = fmaxf(mx, p[f]); mn = fminf(mn, p[f]); }
          float e1[18], e2[18], s1 = 0.f, s2 = 0.f;
#pragma unroll
          for (int f = 0; f < 18; ++f) { e1[f] = __expf(p[f] - mx); s1 += e1[f]; e2[f] = __expf(mn - p[f]); s2 += e2[f]; }
          float r1 = 1.f / s1, r2 = 1.f / s2;
#pragma unroll
          for (int i = 0; i < 9; ++i) { pr[i] = pack2(e1[2 * i] * r1, e1[2 * i + 1] * r1); pr[9 + i] = pack2(e2[2 * i] * r2, e2[2 * i + 1] * r2); }
        };
        finish(p0, pr0);
        finish(p1, pr1);
      }
    }
    __syncthreads();
    {
      typedef LDS_AS s16x4* lp;
      f32x4 kvacc[3][2];
#pragma unroll
      for (int a = 0; a < 3; ++a) for (int b = 0; b < 2; ++b) kvacc[a][b] = f32x4{0.f, 0.f, 0.f, 0.f};
      const int q = fr >> 2, p = lane & 3;
#pragma unroll
      for (int ks = 0; ks < 4; ++ks) {
        bf16x8 pa[3], vb[2];
        int r0 = ks * 32 + 8 * fq + q;
#pragma unroll
        for (int mt = 0; mt < 3; ++mt) {
          const char* a0 = Pk + r0 * PK_PITCH + (wid * 36 + mt * 16 + 4 * p) * 2;
          s16x4 lo = __builtin_amdgcn_ds_read_tr16_b64_v4i16((lp)(a0));
          s16x4 hi = __builtin_amdgcn_ds_read_tr16_b64_v4i16((lp)(a0 + 4 * PK_PITCH));
          pa[mt] = __builtin_shufflevector(lo, hi, 0, 1, 2, 3, 4, 5, 6, 7);
        }
#pragma unroll
        for (int nt = 0; nt < 2; ++nt) {
          const char* b0 = Vt + r0 * VT_PITCH + (wid * 20 + nt * 16 + 4 * p) * 2;
          s16x4 lo = __builtin_amdgcn_ds_read_tr16_b64_v4i16((lp)(b0));
          s16x4 hi = __builtin_amdgcn_ds_read_tr16_b64_v4i16((lp)(b0 + 4 * VT_PITCH));
          vb[nt] = __builtin_shufflevector(lo, hi, 0, 1, 2, 3, 4, 5, 6, 7);
        }
#pragma unroll
        for (int mt = 0; mt < 3; ++mt)
#pragma unroll
          for (int nt = 0; nt < 2; ++nt) kvacc[mt][nt] = __builtin_amdgcn_mfma_f32_16x16x32_bf16(pa[mt], vb[nt], kvacc[mt][nt], 0, 0, 0);
      }
      float* dst = kvslab + ((size_t)rt * 32 + ct * 8 + wid) * 720;
#pragma unroll
      for (int mt = 0; mt < 3; ++mt)
#pragma unroll
        for (int nt = 0; nt < 2; ++nt)
#pragma unroll
          for (int e = 0; e < 4; ++e) {
            int f = mt * 16 + fq * 4 + e, d = nt * 16 + fr;
            if (f < 36 && d < 19) dst[f * 20 + d] = kvacc[mt][nt][e];
          }
    }
  }
}

__global__ __launch_bounds__(512, 2) void k_l1(const u16* __restrict__ xn, const u16* __restrict__ w1, const u16* __restrict__ wqkv,
                                               const u16* __restrict__ wphi, u16* __restrict__ hbuf, u16* __restrict__ qb, float* __restrict__ kvslab) {
  __shared__ __attribute__((aligned(16))) char smem[163840];
  int bid = blockIdx.x, xcd = bid & 7, idx = bid >> 3;
  int rt = (xcd & 1) * 8 + (idx & 7);
  int cs = (xcd >> 1) * 4 + (idx >> 3);
  if (cs < 12) l1_wg<0>(smem, xn, w1, wqkv, wphi, hbuf, qb, kvslab, rt, cs);
  else l1_wg<1>(smem, xn, w1, wqkv, wphi, hbuf, qb, kvslab, rt, cs - 12);
  STAMPK(1, 3);
}

constexpr int G2_STAGE = 17408, G2_NS = 8;
__device__ __forceinline__ int swap23(int r) { return (r & ~0xC) | ((r & 4) << 1) | ((r & 8) >> 1); }

template <int KID>
__device__ __forceinline__ void g2_main(int NIT, char* smem, const u16* __restrict__ A, int lda, const u16* __restrict__ B, int ldb,
                                        f32x4 (&acc)[2][5], int nsw = 1 << 30, const u16* __restrict__ A1 = nullptr, int lda1 = 0, const u16* __restrict__ B1 = nullptr) {
  const int tid = threadIdx.x, lane = tid & 63;
  const int wid = __builtin_amdgcn_readfirstlane(tid >> 6);
  const int wr = wid >> 1, wc = wid & 1, fr = lane & 15, fq = lane >> 4;
  const u16* ag = A + (size_t)(wid * 16 + (lane >> 2)) * lda + (((lane & 3) ^ ((-(lane >> 4)) & 3)) * 8);
  const u16* bg0; const u16* bg1;
  { int ci = wid * 64 + lane; int rho = ci / 18, c = ci - rho * 18; bg0 = B + (size_t)swap23(rho) * ldb + c * 8; }
  { int ci = 8 * 64 + lane; int rho = ci / 18, c = ci - rho * 18; bg1 = B + (size_t)swap23(rho) * ldb + c * 8; }
  const size_t bstep = (size_t)32 * ldb;
  int nissued = 0;
  auto issue = [&](int sb_off) {
    if (nissued == nsw) {
      ag = A1 + (size_t)(wid * 16 + (lane >> 2)) * lda1 + (((lane & 3) ^ ((-(lane >> 4)) & 3)) * 8);
      { int ci = wid * 64 + lane; int rho = ci / 18, c = ci - rho * 18; bg0 = B1 + (size_t)swap23(rho) * ldb + c * 8; }
      { int ci = 8 * 64 + lane; int rho = ci / 18, c = ci - rho * 18; bg1 = B1 + (size_t)swap23(rho) * ldb + c * 8; }
    }
    ++nissued;
    char* sb = smem + sb_off;
    GLDS(ag, sb + wid * 1024); ag += 32;
    GLDS(bg0, sb + 8192 + wid * 1024); bg0 += bstep;
    if (wid == 0) { GLDS(bg1, sb + 8192 + 8192); } bg1 += bstep;
  };
  const int a_base = (wr * 32 + fr) * 64 + ((fq ^ ((-(fr >> 2)) & 3)) * 16);
  const int b_base = 8192 + (16 * (fq >> 1) + 4 * (fq & 1) + (fr >> 2)) * 288 + (2 * (5 * wc) + ((lane & 3) >> 1)) * 16 + 8 * (lane & 1);
  bf16x8 afA[2], bfA[5], afB[2], bfB[5];
  auto ldb_frag = [&](const char* sb, int j) -> bf16x8 {
    typedef LDS_AS s16x4* lp;
    s16x4 lo = __builtin_amdgcn_ds_read_tr16_b64_v4i16((lp)(sb + b_base + j * 32));
    s16x4 hi = __builtin_amdgcn_ds_read_tr16_b64_v4i16((lp)(sb + b_base + j * 32 + 8 * 288));
    return __builtin_shufflevector(lo, hi, 0, 1, 2, 3, 4, 5, 6, 7);
  };
  auto ldfr = [&](int stg, bf16x8 (&af)[2], bf16x8 (&bf)[5]) {
    const char* nb = smem + (stg & (G2_NS - 1)) * G2_STAGE;
#pragma unroll
    for (int j = 0; j < 5; ++j) bf[j] = ldb_frag(nb, j);
#pragma unroll
    for (int m = 0; m < 2; ++m) af[m] = *(const bf16x8*)(nb + a_base + m * 1024);
  };
  auto mma = [&](bf16x8 (&af)[2], bf16x8 (&bf)[5]) {
#pragma unroll
    for (int j = 0; j < 5; ++j)
#pragma unroll
      for (int m = 0; m < 2; ++m)
        acc[m][j] = __builtin_amdgcn_mfma_f32_16x16x32_bf16(bf[j], af[m], acc[m][j], 0, 0, 0);
  };
#define G2_IL() do { _Pragma("unroll") for (int _i = 0; _i < 6; ++_i) { __builtin_amdgcn_sched_group_barrier(0x100, 2, 0); __builtin_amdgcn_sched_group_barrier(0x8, 1, 0); } __builtin_amdgcn_sched_group_barrier(0x8, 8, 0); } while (0)
  STAMPK(KID, 0);
#pragma unroll
  for (int s = 0; s < G2_NS; ++s) issue(s * G2_STAGE);
  if (wid == 0) WAIT_V(21); else WAIT_V(14);
  BAR();
  STAMPK(KID, 1);
  ldfr(0, afA, bfA);
#pragma unroll 1
  for (int t = 0; t < NIT - G2_NS; t += 2) {
    if (wid == 0) WAIT_V(18); else WAIT_V(12);
    WAIT_L0(); BAR();
    issue((t & (G2_NS - 1)) * G2_STAGE);
    ldfr(t + 1, afB, bfB); mma(afA, bfA); G2_IL();
    if (wid == 0) WAIT_V(18); else WAIT_V(12);
    WAIT_L0(); BAR();
    issue(((t + 1) & (G2_NS - 1)) * G2_STAGE);
    ldfr(t + 2, afA, bfA); mma(afB, bfB); G2_IL();
  }
#define G2_TAIL(R, N3, N2, X, Y) { if (wid == 0) WAIT_V(N3); else WAIT_V(N2); WAIT_L0(); BAR(); ldfr(NIT - G2_NS + R + 1, af##Y, bf##Y); mma(af##X, bf##X); G2_IL(); }
  G2_TAIL(0, 18, 12, A, B) G2_TAIL(1, 15, 10, B, A) G2_TAIL(2, 12, 8, A, B) G2_TAIL(3, 9, 6, B, A) G2_TAIL(4, 6, 4, A, B) G2_TAIL(5, 3, 2, B, A) G2_TAIL(6, 0, 0, A, B)
  mma(afB, bfB);
  STAMPK(KID, 2);
}

__global__ __launch_bounds__(512, 2) void k_g2(const u16* __restrict__ hbuf, const u16* __restrict__ w2, const u16* __restrict__ o, const u16* __restrict__ wo,
                                               float* __restrict__ slab) {
  __shared__ __attribute__((aligned(16))) char smem[G2_NS * G2_STAGE + 64];
  int bid = blockIdx.x, xcd = bid & 7, idx = bid >> 3;
  int ks = xcd >> 1, rt = (xcd & 1) * 8 + (idx & 7), ct = idx >> 3;
  f32x4 acc[2][5];
#pragma unroll
  for (int m = 0; m < 2; ++m)
#pragma unroll
    for (int j = 0; j < 5; ++j) acc[m][j] = f32x4{0.f, 0.f, 0.f, 0.f};
  int st0 = ks == 0 ? 0 : 2 + 40 * ks;
  int nit = ks == 0 ? 42 : 40;
  g2_main<2>(nit, smem, hbuf + (size_t)rt * 128 * F + st0 * 32, F, w2 + (size_t)st0 * 32 * D + ct * 144, D, acc,
             ks == 3 ? 22 : (1 << 30), o + (size_t)rt * 128 * D, D, wo + ct * 144);
  const int tid = threadIdx.x, lane = tid & 63, wid = tid >> 6, wr = wid >> 1, wc = wid & 1, fr = lane & 15, fq = lane >> 4;
  float* dst = slab + (size_t)ks * S * D;
#pragma unroll
  for (int m = 0; m < 2; ++m)
#pragma unroll
    for (int j = 0; j < 5; ++j) {
      int T = 5 * wc + j;
      if (T < 9) *(f32x4*)(dst + (size_t)(rt * 128 + wr * 32 + m * 16 + fr) * D + ct * 144 + T * 16 + fq * 4) = acc[m][j];
    }
  STAMPK(2, 3);
}

__global__ __launch_bounds__(256) void k_l3(const float* __restrict__ slab, const u16* __restrict__ x, u16* __restrict__ out) {
  int i = (blockIdx.x * 256 + threadIdx.x) * 4;
  f32x4 v = *(const f32x4*)(slab + i);
#pragma unroll
  for (int k = 1; k < 4; ++k) v += *(const f32x4*)(slab + (size_t)k * S * D + i);
  uint2 xv = *(const uint2*)(x + i);
  float x0 = __uint_as_float(xv.x << 16), x1 = __uint_as_float(xv.x & 0xffff0000u), x2 = __uint_as_float(xv.y << 16), x3 = __uint_as_float(xv.y & 0xffff0000u);
  *(uint2*)(out + i) = make_uint2(pack2(v[0] + x0, v[1] + x1), pack2(v[2] + x2, v[3] + x3));
}

__global__ __launch_bounds__(256) void k_qo2(const u16* __restrict__ qb, const u16* __restrict__ wphi, const float* __restrict__ kvslab, u16* __restrict__ o) {
  __shared__ float kv[720];
  __shared__ float W[324];
  int h = blockIdx.x >> 3, rg = blockIdx.x & 7, tid = threadIdx.x;
  for (int i = tid; i < 720; i += 256) {
    float s = 0.f;
#pragma unroll
    for (int r = 0; r < 16; ++r) s += kvslab[((size_t)r * 32 + h) * 720 + i];
    kv[i] = bf2f(f2bf(s));
  }
  for (int i = tid; i < 324; i += 256) W[i] = bf2f(wphi[h * 324 + i]);
  int row = rg * 256 + tid;
  const unsigned* src = (const unsigned*)(qb + (size_t)row * D + h * DH);
  float xq[18];
#pragma unroll
  for (int i = 0; i < 9; ++i) { unsigned u = src[i]; xq[2 * i] = __uint_as_float(u << 16); xq[2 * i + 1] = __uint_as_float(u & 0xffff0000u); }
  __syncthreads();
  float p[18];
#pragma unroll
  for (int f = 0; f < 18; ++f) p[f] = 0.f;
#pragma unroll
  for (int d = 0; d < 18; ++d)
#pragma unroll
    for (int f = 0; f < 18; ++f) p[f] = fmaf(xq[d], W[d * 18 + f], p[f]);
  float mx = -1e30f, mn = 1e30f;
#pragma unroll
  for (int f = 0; f < 18; ++f) { p[f] = bf2f(f2bf(p[f])); mx = fmaxf(mx, p[f]); mn = fminf(mn, p[f]); }
  float phi[36], s1 = 0.f, s2 = 0.f;
#pragma unroll
  for (int f = 0; f < 18; ++f) { phi[f] = __expf(p[f] - mx); s1 += phi[f]; phi[18 + f] = __expf(mn - p[f]); s2 += phi[18 + f]; }
  float r1 = 1.f / s1, r2 = 1.f / s2;
#pragma unroll
  for (int f = 0; f < 18; ++f) { phi[f] = bf2f(f2bf(phi[f] * r1)); phi[18 + f] = bf2f(f2bf(phi[18 + f] * r2)); }
  float num[19];
#pragma unroll
  for (int d = 0; d < 19; ++d) num[d] = 0.f;
#pragma unroll
  for (int f = 0; f < 36; ++f)
#pragma unroll
    for (int d = 0; d < 19; ++d) num[d] = fmaf(phi[f], kv[f * 20 + d], num[d]);
  float den = bf2f(f2bf(num[18])) + 1e-6f;
  float rd = 1.f / den;
  unsigned* dst = (unsigned*)(o + (size_t)row * D + h * DH);
#pragma unroll
  for (int i = 0; i < 9; ++i) dst[i] = pack2(bf2f(f2bf(num[2 * i])) * rd, bf2f(f2bf(num[2 * i + 1])) * rd);
}

extern "C" void kernel_launch(void* const* d_in, const int* in_sizes, int n_in, void* d_out, int out_size, void* d_ws, size_t ws_size, hipStream_t stream) {
  const u16* x = (const u16*)d_in[0]; const u16* wqkv = (const u16*)d_in[1]; const u16* wphi = (const u16*)d_in[2]; const u16* wo = (const u16*)d_in[3];
  const u16* w1 = (const u16*)d_in[5]; const u16* w2 = (const u16*)d_in[6];
  u16* out = (u16*)d_out;
  char* ws = (char*)d_ws;
  u16* xn = (u16*)ws; ws += (size_t)S * D * 2;
  u16* qb = (u16*)ws; ws += (size_t)S * D * 2;
  u16* hbuf = (u16*)ws; ws += (size_t)S * F * 2;
  float* kvslab = (float*)ws; ws += 16 * 32 * 720 * 4;
  float* kv = (float*)ws; ws += 32 * 720 * 4;
  u16* o = (u16*)ws; ws += (size_t)S * D * 2;
  float* slab = (float*)ws; ws += (size_t)4 * S * D * 4;
  k_ln<<<S / 4, 256, 0, stream>>>(x, xn);
  k_l1<<<256, 512, 0, stream>>>(xn, w1, wqkv, wphi, hbuf, qb, kvslab);
  k_qo2<<<256, 256, 0, stream>>>(qb, wphi, kvslab, o);
  k_g2<<<256, 512, 0, stream>>>(hbuf, w2, o, wo, slab);
  k_l3<<<S * D / 1024, 256, 0, stream>>>(slab, x, out);
}
```

```cpp
#include <hip/hip_runtime.h>
#include <hip/hip_bf16.h>
#include <stdint.h>

typedef __attribute__((ext_vector_type(8))) short bf16x8;
typedef __attribute__((ext_vector_type(4))) short s16x4;
typedef __attribute__((ext_vector_type(4))) float f32x4;
typedef unsigned short u16;
#define LDS_AS __attribute__((address_space(3)))

__device__ __forceinline__ float bf2f(u16 v) { return __uint_as_float(((unsigned)v) << 16); }
__device__ __forceinline__ u16 f2bf(float f) {
  unsigned u = __float_as_uint(f);
  u += 0x7fffu + ((u >> 16) & 1u);
  return (u16)(u >> 16);
}
__device__ __forceinline__ unsigned pack2(float lo, float hi) { unsigned r; asm("v_cvt_pk_bf16_f32 %0, %1, %2" : "=v"(r) : "v"(lo), "v"(hi)); return r; }
__device__ __forceinline__ float rbf(float f) { return __uint_as_float(pack2(f, f) << 16); }

constexpr int S = 2048, D = 576, H = 32, DH = 18, F = 4608;

#define STAMPK(k, i)
__global__ __launch_bounds__(256) void k_ln(const u16* __restrict__ x, u16* __restrict__ xn, const u16* __restrict__ wqkv, const u16* __restrict__ wphi, u16* __restrict__ wf) {
  if (blockIdx.x >= S / 4) {
    int t = (blockIdx.x - S / 4) * 256 + threadIdx.x;
    int k = t >> 6, wh = t & 63, h = wh & 31;
    const u16* src = wqkv + (size_t)k * (3 * D) + wh * 18;
    const u16* W = wphi + h * 324;
    float a[18];
#pragma unroll
    for (int d = 0; d < 18; ++d) a[d] = bf2f(src[d]);
    u16* dst = wf + (size_t)k * (2 * D) + wh * 18;
#pragma unroll 2
    for (int f = 0; f < 18; f += 2) {
      float s0 = 0.f, s1 = 0.f;
#pragma unroll
      for (int d = 0; d < 18; ++d) { s0 = fmaf(a[d], bf2f(W[d * 18 + f]), s0); s1 = fmaf(a[d], bf2f(W[d * 18 + f + 1]), s1); }
      *(unsigned*)(dst + f) = pack2(s0, s1);
    }
    return;
  }
  int row = blockIdx.x * 4 + (threadIdx.x >> 6), lane = threadIdx.x & 63;
  const u16* xr = x + (size_t)row * D;
  float v[9]; float s = 0.f;
  for (int i = 0; i < 9; ++i) { v[i] = bf2f(xr[lane + 64 * i]); s += v[i]; }
  for (int o = 32; o; o >>= 1) s += __shfl_xor(s, o);
  float m = s * (1.f / D);
  float q = 0.f;
  for (int i = 0; i < 9; ++i) { float d = v[i] - m; q += d * d; }
  for (int o = 32; o; o >>= 1) q += __shfl_xor(q, o);
  float r = rsqrtf(q * (1.f / D) + 1e-5f);
  for (int i = 0; i < 9; ++i) xn[(size_t)row * D + lane + 64 * i] = f2bf((v[i] - m) * r);
}

__device__ __forceinline__ void glds16_asm(const void* gsrc, unsigned lds_dst) {
  unsigned keep;
  asm volatile("s_mov_b32 %0, m0\n\ts_mov_b32 m0, %2\n\ts_nop 0\n\tglobal_load_lds_dwordx4 %1, off\n\ts_mov_b32 m0, %0" : "=&s"(keep) : "v"(gsrc), "s"(lds_dst) : "memory");
}
#define GLDS(g, l) glds16_asm((const void*)(g), (unsigned)(size_t)(l))
#define WAIT_V(n) asm volatile("s_waitcnt vmcnt(" #n ")" ::: "memory")
#define WAIT_L0() asm volatile("s_waitcnt lgkmcnt(0)" ::: "memory")
#define BAR() __builtin_amdgcn_s_barrier()

__device__ __forceinline__ int swap23(int r) { return (r & ~0xC) | ((r & 4) << 1) | ((r & 8) >> 1); }

template <int N> __device__ __forceinline__ void wait_v() { asm volatile("s_waitcnt vmcnt(%0)" :: "n"(N) : "memory"); }
template <int MT, int NTW, int NCH, int NS>
struct Core {
  static constexpr int PITCH = NCH * 16;
  static constexpr int NB = (32 * NCH + 63) / 64;
  static constexpr int STAGE = 8192 + NB * 1024;
  static constexpr int NLO = 1 + NB / 8, NHI = 1 + (NB + 7) / 8, XW = NB % 8;
  template <int K> static __device__ __forceinline__ void wait_stages(int wid) {
    if (XW == 0) wait_v<K * NLO>();
    else { if (wid < XW) wait_v<K * NHI>(); else wait_v<K * NLO>(); }
  }
};

template <int MT, int NTW, int NCH, int NS, int KID, class BSRC>
__device__ __forceinline__ void gemm_core(int NIT, char* smem, const u16* ag0, BSRC&& bsrc, f32x4 (&acc)[MT][NTW], int a_row0  , int tile0  ,
                                          int nsw, const u16* ag1) {
  using C = Core<MT, NTW, NCH, NS>;
  const int tid = threadIdx.x, lane = tid & 63;
  const int wid = __builtin_amdgcn_readfirstlane(tid >> 6);
  const int fr = lane & 15, fq = lane >> 4;
  const u16* ag = ag0;
  const u16* bg[4]; int bst[4];
#pragma unroll
  for (int i = 0; i < 4; ++i) {
    int ci = (wid + 8 * i) * 64 + lane;
    int rho = ci / NCH, c = ci - rho * NCH;
    if (rho > 31) { rho = 31; }
    bsrc(swap23(rho), c, bg[i], bst[i], 0);
  }
  int nissued = 0;
  auto issue = [&](int stg) {
    if (nissued == nsw) {
      ag = ag1;
#pragma unroll
      for (int i = 0; i < 4; ++i) {
        int ci = (wid + 8 * i) * 64 + lane;
        int rho = ci / NCH, c = ci - rho * NCH;
        if (rho > 31) { rho = 31; }
        bsrc(swap23(rho), c, bg[i], bst[i], 1);
      }
    }
    ++nissued;
    char* sb = smem + (stg % NS) * C::STAGE;
    GLDS(ag, sb + wid * 1024); ag += 32;
#pragma unroll
    for (int i = 0; i < 4; ++i) {
      if (8 * i < C::NB && (8 * i + 8 <= C::NB || wid < C::NB - 8 * i)) GLDS(bg[i], sb + 8192 + (wid + 8 * i) * 1024);
      bg[i] += bst[i];
    }
  };
  const int a_base = (a_row0 + fr) * 64 + ((fq ^ ((-(fr >> 2)) & 3)) * 16);
  const int b_base = 8192 + (16 * (fq >> 1) + 4 * (fq & 1) + (fr >> 2)) * C::PITCH + (2 * tile0 + ((lane & 3) >> 1)) * 16 + 8 * (lane & 1);
  bf16x8 afA[MT], bfA[NTW], afB[MT], bfB[NTW];
  auto ldfr = [&](int stg, bf16x8 (&af)[MT], bf16x8 (&bf)[NTW]) {
    typedef LDS_AS s16x4* lp;
    const char* nb = smem + (stg % NS) * C::STAGE;
#pragma unroll
    for (int j = 0; j < NTW; ++j) {
      s16x4 lo = __builtin_amdgcn_ds_read_tr16_b64_v4i16((lp)(nb + b_base + j * 32));
      s16x4 hi = __builtin_amdgcn_ds_read_tr16_b64_v4i16((lp)(nb + b_base + j * 32 + 8 * C::PITCH));
      bf[j] = __builtin_shufflevector(lo, hi, 0, 1, 2, 3, 4, 5, 6, 7);
    }
#pragma unroll
    for (int m = 0; m < MT; ++m) af[m] = *(const bf16x8*)(nb + a_base + m * 1024);
  };
  auto mma = [&](bf16x8 (&af)[MT], bf16x8 (&bf)[NTW]) {
#pragma unroll
    for (int j = 0; j < NTW; ++j)
#pragma unroll
      for (int m = 0; m < MT; ++m)
        acc[m][j] = __builtin_amdgcn_mfma_f32_16x16x32_bf16(bf[j], af[m], acc[m][j], 0, 0, 0);
  };
  constexpr int NRD = 2 * NTW + MT, NMM = MT * NTW;
  constexpr int MPR = NMM / NRD > 0 ? NMM / NRD : 1;
#define CORE_IL() do { _Pragma("unroll") for (int _i = 0; _i < NRD; ++_i) { __builtin_amdgcn_sched_group_barrier(0x100, 1, 0); __builtin_amdgcn_sched_group_barrier(0x8, MPR, 0); } __builtin_amdgcn_sched_group_barrier(0x8, 64, 0); } while (0)
  STAMPK(KID, 0);
#pragma unroll
  for (int s = 0; s < NS - 1; ++s) issue(s);
  C::template wait_stages<NS - 2>(wid);
  BAR();
  STAMPK(KID, 1);
  ldfr(0, afA, bfA);
#pragma unroll 1
  for (int t = 0; t + NS < NIT; t += 2) {
    C::template wait_stages<NS - 3>(wid); BAR();
    issue(t + NS - 1);
    ldfr(t + 1, afB, bfB); mma(afA, bfA); CORE_IL();
    C::template wait_stages<NS - 3>(wid); BAR();
    issue(t + NS);
    ldfr(t + 2, afA, bfA); mma(afB, bfB); CORE_IL();
  }
  static_assert(NS == 4 || NS == 8, "NS");
  C::template wait_stages<NS - 3>(wid); BAR();
  issue(NIT - 1);
  ldfr(NIT - NS + 1, afB, bfB); mma(afA, bfA); CORE_IL();
  if (NS == 8) {
    C::template wait_stages<5>(wid); BAR(); ldfr(NIT - 6, afA, bfA); mma(afB, bfB); CORE_IL();
    C::template wait_stages<4>(wid); BAR(); ldfr(NIT - 5, afB, bfB); mma(afA, bfA); CORE_IL();
    C::template wait_stages<3>(wid); BAR(); ldfr(NIT - 4, afA, bfA); mma(afB, bfB); CORE_IL();
    C::template wait_stages<2>(wid); BAR(); ldfr(NIT - 3, afB, bfB); mma(afA, bfA); CORE_IL();
  }
  C::template wait_stages<1>(wid); BAR(); ldfr(NIT - 2, afA, bfA); mma(afB, bfB); CORE_IL();
  C::template wait_stages<0>(wid); BAR(); ldfr(NIT - 1, afB, bfB); mma(afA, bfA); CORE_IL();
  mma(afB, bfB);
  STAMPK(KID, 2);
}

#define SCHED_IL() do { _Pragma("unroll") for (int _i = 0; _i < 9; ++_i) { __builtin_amdgcn_sched_group_barrier(0x100, 2, 0); __builtin_amdgcn_sched_group_barrier(0x8, 1, 0); } __builtin_amdgcn_sched_group_barrier(0x8, 32, 0); } while (0)

__device__ __forceinline__ bf16x8 tr_frag(const char* p) {
  typedef LDS_AS s16x4* lp;
  s16x4 lo = __builtin_amdgcn_ds_read_tr16_b64_v4i16((lp)(p));
  s16x4 hi = __builtin_amdgcn_ds_read_tr16_b64_v4i16((lp)(p + 4096));
  return __builtin_shufflevector(lo, hi, 0, 1, 2, 3, 4, 5, 6, 7);
}

template <int TYPE>
__device__ __forceinline__ void l1_wg(char* smem, const u16* __restrict__ xn, const u16* __restrict__ w1, const u16* __restrict__ wqkv,
                                      const u16* __restrict__ wf, u16* __restrict__ hbuf, u16* __restrict__ qb, float* __restrict__ kvslab,
                                      int rt, int ct) {
  constexpr int NCH = TYPE ? 38 : 54;
  constexpr int NTW = TYPE ? 5 : 7;
  const int tid = threadIdx.x, lane = tid & 63;
  const int wid = __builtin_amdgcn_readfirstlane(tid >> 6);
  const int wr = wid >> 2, wc = wid & 3, fr = lane & 15, fq = lane >> 4;
  const u16* ag = xn + (size_t)(rt * 128 + wid * 16 + (lane >> 2)) * D + (((lane & 3) ^ ((-(lane >> 4)) & 3)) * 8);
  f32x4 acc[4][NTW];
#pragma unroll
  for (int m = 0; m < 4; ++m)
#pragma unroll
    for (int j = 0; j < NTW; ++j) acc[m][j] = f32x4{0.f, 0.f, 0.f, 0.f};
  auto bsrc = [&](int k, int c, const u16*& p, int& st, int phase) {
    if (TYPE == 0) {
      int gc = ct * 432 + c * 8;
      if (gc < F) { p = w1 + (size_t)k * F + gc; st = 32 * F; }
      else { p = wf + (size_t)k * (2 * D) + (gc - F); st = 32 * 2 * D; }
    } else {
      if (c > 35) c = 35;
      int part = c >= 18 ? 1 : 0, cc = c - 18 * part;
      if (part == 0) { p = wf + (size_t)k * (2 * D) + D + ct * 144 + cc * 8; st = 32 * 2 * D; }
      else { p = wqkv + (size_t)k * (3 * D) + 2 * D + ct * 144 + cc * 8; st = 32 * 3 * D; }
    }
  };
  gemm_core<4, NTW, NCH, 4, 1>(18, smem, ag, bsrc, acc, wr * 64, NTW * wc, 1 << 30, nullptr);
  if (TYPE == 0) {
#pragma unroll
    for (int m = 0; m < 4; ++m)
#pragma unroll
      for (int j = 0; j < NTW; ++j) {
        int T = NTW * wc + j;
        if (T < 27) {
          int row = rt * 128 + wr * 64 + m * 16 + fr;
          int col0 = ct * 432 + T * 16 + fq * 4;
          f32x4 v = acc[m][j];
          if (col0 < F) {
            float o[4];
#pragma unroll
            for (int e = 0; e < 4; ++e) o[e] = v[e] * __frcp_rn(1.f + __expf(-v[e]));
            uint2 pk = make_uint2(pack2(o[0], o[1]), pack2(o[2], o[3]));
            *(uint2*)(hbuf + (size_t)row * F + col0) = pk;
          } else {
            uint2 pk = make_uint2(pack2(v[0], v[1]), pack2(v[2], v[3]));
            *(uint2*)(qb + (size_t)row * D + (col0 - F)) = pk;
          }
        }
      }
  } else {
    constexpr int PK_PITCH = 608, VT_PITCH = 352;
    char* Pk = smem;
    char* Vt = smem + 128 * PK_PITCH;
    __syncthreads();
#pragma unroll
    for (int m = 0; m < 4; ++m)
#pragma unroll
      for (int j = 0; j < NTW; ++j) {
        int T = NTW * wc + j;
        if (T < 18) {
          int row = wr * 64 + m * 16 + fr;
#pragma unroll
          for (int e = 0; e < 4; ++e) {
            int c = T * 16 + fq * 4 + e;
            int part = c >= 144 ? 1 : 0, cc = c - 144 * part, hd = cc / 18, d = cc - hd * 18;
            u16 val = f2bf(acc[m][j][e]);
            if (part == 0) *(u16*)(Pk + row * PK_PITCH + (hd * 36 + d) * 2) = val;
            else *(u16*)(Vt + row * VT_PITCH + (hd * 20 + d) * 2) = val;
          }
        }
      }
    for (int i = tid; i < 128 * 8; i += 512) { int row = i >> 3, hd = i & 7; *(u16*)(Vt + row * VT_PITCH + (hd * 20 + 18) * 2) = 0x3f80; }
    __syncthreads();
    {
      {
        unsigned* pr0 = (unsigned*)(Pk + lane * PK_PITCH + wid * 72);
        unsigned* pr1 = (unsigned*)(Pk + (lane + 64) * PK_PITCH + wid * 72);
        float p0[18], p1[18];
#pragma unroll
        for (int i = 0; i < 9; ++i) {
          unsigned u = pr0[i]; p0[2 * i] = __uint_as_float(u << 16); p0[2 * i + 1] = __uint_as_float(u & 0xffff0000u);
          unsigned w = pr1[i]; p1[2 * i] = __uint_as_float(w << 16); p1[2 * i + 1] = __uint_as_float(w & 0xffff0000u);
        }
        auto finish = [&](float* p, unsigned* pr) {
          float mx = -1e30f, mn = 1e30f;
#pragma unroll
          for (int f = 0; f < 18; ++f) { mx = fmaxf(mx, p[f]); mn = fminf(mn, p[f]); }
          float e1[18], e2[18], s1 = 0.f, s2 = 0.f;
#pragma unroll
          for (int f = 0; f < 18; ++f) { e1[f] = __expf(p[f] - mx); s1 += e1[f]; e2[f] = __expf(mn - p[f]); s2 += e2[f]; }
          float r1 = 1.f / s1, r2 = 1.f / s2;
#pragma unroll
          for (int i = 0; i < 9; ++i) { pr[i] = pack2(e1[2 * i] * r1, e1[2 * i + 1] * r1); pr[9 + i] = pack2(e2[2 * i] * r2, e2[2 * i + 1] * r2); }
        };
        finish(p0, pr0);
        finish(p1, pr1);
      }
    }
    __syncthreads();
    {
      typedef LDS_AS s16x4* lp;
      f32x4 kvacc[3][2];
#pragma unroll
      for (int a = 0; a < 3; ++a) for (int b = 0; b < 2; ++b) kvacc[a][b] = f32x4{0.f, 0.f, 0.f, 0.f};
      const int q = fr >> 2, p = lane & 3;
#pragma unroll
      for (int ks = 0; ks < 4; ++ks) {
        bf16x8 pa[3], vb[2];
        int r0 = ks * 32 + 8 * fq + q;
#pragma unroll
        for (int mt = 0; mt < 3; ++mt) {
          const char* a0 = Pk + r0 * PK_PITCH + (wid * 36 + mt * 16 + 4 * p) * 2;
          s16x4 lo = __builtin_amdgcn_ds_read_tr16_b64_v4i16((lp)(a0));
          s16x4 hi = __builtin_amdgcn_ds_read_tr16_b64_v4i16((lp)(a0 + 4 * PK_PITCH));
          pa[mt] = __builtin_shufflevector(lo, hi, 0, 1, 2, 3, 4, 5, 6, 7);
        }
#pragma unroll
        for (int nt = 0; nt < 2; ++nt) {
          const char* b0 = Vt + r0 * VT_PITCH + (wid * 20 + nt * 16 + 4 * p) * 2;
          s16x4 lo = __builtin_amdgcn_ds_read_tr16_b64_v4i16((lp)(b0));
          s16x4 hi = __builtin_amdgcn_ds_read_tr16_b64_v4i16((lp)(b0 + 4 * VT_PITCH));
          vb[nt] = __builtin_shufflevector(lo, hi, 0, 1, 2, 3, 4, 5, 6, 7);
        }
#pragma unroll
        for (int mt = 0; mt < 3; ++mt)
#pragma unroll
          for (int nt = 0; nt < 2; ++nt) kvacc[mt][nt] = __builtin_amdgcn_mfma_f32_16x16x32_bf16(pa[mt], vb[nt], kvacc[mt][nt], 0, 0, 0);
      }
      float* dst = kvslab + ((size_t)rt * 32 + ct * 8 + wid) * 720;
#pragma unroll
      for (int mt = 0; mt < 3; ++mt)
#pragma unroll
        for (int nt = 0; nt < 2; ++nt)
#pragma unroll
          for (int e = 0; e < 4; ++e) {
            int f = mt * 16 + fq * 4 + e, d = nt * 16 + fr;
            if (f < 36 && d < 19) dst[f * 20 + d] = kvacc[mt][nt][e];
          }
    }
  }
}

__global__ __launch_bounds__(512, 2) void k_l1(const u16* __restrict__ xn, const u16* __restrict__ w1, const u16* __restrict__ wqkv,
                                               const u16* __restrict__ wf, u16* __restrict__ hbuf, u16* __restrict__ qb, float* __restrict__ kvslab) {
  __shared__ __attribute__((aligned(16))) char smem[163840];
  int bid = blockIdx.x, xcd = bid & 7, idx = bid >> 3;
  int rt = (xcd & 1) * 8 + (idx & 7);
  int cs = (xcd >> 1) * 4 + (idx >> 3);
  if (cs < 12) l1_wg<0>(smem, xn, w1, wqkv, wf, hbuf, qb, kvslab, rt, cs);
  else l1_wg<1>(smem, xn, w1, wqkv, wf, hbuf, qb, kvslab, rt, cs - 12);
  STAMPK(1, 3);
}

constexpr int G2_NS = 8, G2_STAGE = 17408;
__global__ __launch_bounds__(512, 2) void k_g2(const u16* __restrict__ hbuf, const u16* __restrict__ w2, const u16* __restrict__ o, const u16* __restrict__ wo,
                                               float* __restrict__ slab) {
  __shared__ __attribute__((aligned(16))) char smem[G2_NS * G2_STAGE + 64];
  int bid = blockIdx.x, xcd = bid & 7, idx = bid >> 3;
  int ks = xcd >> 1, rt = (xcd & 1) * 8 + (idx & 7), ct = idx >> 3;
  f32x4 acc[2][5];
#pragma unroll
  for (int m = 0; m < 2; ++m)
#pragma unroll
    for (int j = 0; j < 5; ++j) acc[m][j] = f32x4{0.f, 0.f, 0.f, 0.f};
  int st0 = ks == 0 ? 0 : 2 + 40 * ks;
  int nit = ks == 0 ? 42 : 40;
  const int tid = threadIdx.x, lane = tid & 63, wid = tid >> 6, wr = wid >> 1, wc = wid & 1, fr = lane & 15, fq = lane >> 4;
  {
    const int aoff_r = wid * 16 + (lane >> 2), aoff_c = (((lane & 3) ^ ((-(lane >> 4)) & 3)) * 8);
    const u16* ag0 = hbuf + (size_t)(rt * 128 + aoff_r) * F + st0 * 32 + aoff_c;
    const u16* ag1 = o + (size_t)(rt * 128 + aoff_r) * D + aoff_c;
    auto bsrc = [&](int k, int c, const u16*& p, int& st, int phase) {
      st = 32 * D;
      p = phase == 0 ? (w2 + (size_t)(st0 * 32 + k) * D + ct * 144 + c * 8) : (wo + (size_t)k * D + ct * 144 + c * 8);
    };
    gemm_core<2, 5, 18, G2_NS, 2>(nit, smem, ag0, bsrc, acc, wr * 32, 5 * wc, ks == 3 ? 22 : (1 << 30), ag1);
  }
  float* dst = slab + (size_t)ks * S * D;
#pragma unroll
  for (int m = 0; m < 2; ++m)
#pragma unroll
    for (int j = 0; j < 5; ++j) {
      int T = 5 * wc + j;
      if (T < 9) *(f32x4*)(dst + (size_t)(rt * 128 + wr * 32 + m * 16 + fr) * D + ct * 144 + T * 16 + fq * 4) = acc[m][j];
    }
  STAMPK(2, 3);
}

__global__ __launch_bounds__(256) void k_l3(const float* __restrict__ slab, const u16* __restrict__ x, u16* __restrict__ out) {
  int i = (blockIdx.x * 256 + threadIdx.x) * 4;
  f32x4 v = *(const f32x4*)(slab + i);
#pragma unroll
  for (int k = 1; k < 4; ++k) v += *(const f32x4*)(slab + (size_t)k * S * D + i);
  uint2 xv = *(const uint2*)(x + i);
  float x0 = __uint_as_float(xv.x << 16), x1 = __uint_as_float(xv.x & 0xffff0000u), x2 = __uint_as_float(xv.y << 16), x3 = __uint_as_float(xv.y & 0xffff0000u);
  *(uint2*)(out + i) = make_uint2(pack2(v[0] + x0, v[1] + x1), pack2(v[2] + x2, v[3] + x3));
}

__global__ __launch_bounds__(256) void k_qo3(const u16* __restrict__ pq, const float* __restrict__ kvslab, u16* __restrict__ o) {
  __shared__ __attribute__((aligned(16))) u16 kvT[32 * 72];
  const int h = blockIdx.x >> 3, rg = blockIdx.x & 7, tid = threadIdx.x, lane = tid & 63, wid = tid >> 6;
  const int fr = lane & 15, fq = lane >> 4;
  for (int i = tid; i < 32 * 64; i += 256) {
    int n = i >> 6, k = i & 63, s = k >> 5, q = (k >> 3) & 3, j = k & 7;
    int base = q * 5 - (q == 3 ? 1 : 0), cnt = q < 2 ? 5 : 4;
    float v = 0.f;
    if (j < cnt && n < 19) {
      int f = s * 18 + base + j;
#pragma unroll
      for (int r = 0; r < 16; ++r) v += kvslab[((size_t)r * 32 + h) * 720 + f * 20 + n];
    }
    kvT[n * 72 + k] = f2bf(v);
  }
  const int base = fq * 5 - (fq == 3 ? 1 : 0), cnt = fq < 2 ? 5 : 4;
  float p[4][5];
#pragma unroll
  for (int mt = 0; mt < 4; ++mt) {
    const u16* pr = pq + (size_t)(rg * 256 + wid * 64 + mt * 16 + fr) * D + h * DH + base;
#pragma unroll
    for (int j = 0; j < 5; ++j) p[mt][j] = bf2f(pr[j < cnt ? j : 0]);
  }
  __syncthreads();
  bf16x8 bfr[2][2];
#pragma unroll
  for (int s = 0; s < 2; ++s)
#pragma unroll
    for (int j = 0; j < 2; ++j) bfr[s][j] = *(const bf16x8*)(kvT + (j * 16 + fr) * 72 + s * 32 + fq * 8);
#pragma unroll
  for (int mt = 0; mt < 4; ++mt) {
    float mx = p[mt][0], mn = p[mt][0];
#pragma unroll
    for (int j = 1; j < 5; ++j) { mx = fmaxf(mx, p[mt][j]); mn = fminf(mn, p[mt][j]); }
    mx = fmaxf(mx, __shfl_xor(mx, 16)); mx = fmaxf(mx, __shfl_xor(mx, 32));
    mn = fminf(mn, __shfl_xor(mn, 16)); mn = fminf(mn, __shfl_xor(mn, 32));
    float e1[5], e2[5], s1 = 0.f, s2 = 0.f;
#pragma unroll
    for (int j = 0; j < 5; ++j) {
      e1[j] = j < cnt ? __expf(p[mt][j] - mx) : 0.f; e2[j] = j < cnt ? __expf(mn - p[mt][j]) : 0.f;
      s1 += e1[j]; s2 += e2[j];
    }
    s1 += __shfl_xor(s1, 16); s1 += __shfl_xor(s1, 32);
    s2 += __shfl_xor(s2, 16); s2 += __shfl_xor(s2, 32);
    float r1 = 1.f / s1, r2 = 1.f / s2;
    typedef __attribute__((ext_vector_type(4))) unsigned u32x4;
    u32x4 a0 = {pack2(e1[0] * r1, e1[1] * r1), pack2(e1[2] * r1, e1[3] * r1), pack2(e1[4] * r1, 0.f), 0u};
    u32x4 a1 = {pack2(e2[0] * r2, e2[1] * r2), pack2(e2[2] * r2, e2[3] * r2), pack2(e2[4] * r2, 0.f), 0u};
    bf16x8 af0 = __builtin_bit_cast(bf16x8, a0), af1 = __builtin_bit_cast(bf16x8, a1);
    f32x4 acc[2];
#pragma unroll
    for (int j = 0; j < 2; ++j) {
      acc[j] = f32x4{0.f, 0.f, 0.f, 0.f};
      acc[j] = __builtin_amdgcn_mfma_f32_16x16x32_bf16(bfr[0][j], af0, acc[j], 0, 0, 0);
      acc[j] = __builtin_amdgcn_mfma_f32_16x16x32_bf16(bfr[1][j], af1, acc[j], 0, 0, 0);
    }
    float den = rbf(__shfl(acc[1][2], fr)) + 1e-6f;
    float rd = 1.f / den;
    unsigned* dst = (unsigned*)(o + (size_t)(rg * 256 + wid * 64 + mt * 16 + fr) * D + h * DH);
    dst[2 * fq] = pack2(rbf(acc[0][0]) * rd, rbf(acc[0][1]) * rd);
    dst[2 * fq + 1] = pack2(rbf(acc[0][2]) * rd, rbf(acc[0][3]) * rd);
    if (fq == 0) dst[8] = pack2(rbf(acc[1][0]) * rd, rbf(acc[1][1]) * rd);
  }
}

extern "C" void kernel_launch(void* const* d_in, const int* in_sizes, int n_in, void* d_out, int out_size, void* d_ws, size_t ws_size, hipStream_t stream) {
  const u16* x = (const u16*)d_in[0]; const u16* wqkv = (const u16*)d_in[1]; const u16* wphi = (const u16*)d_in[2]; const u16* wo = (const u16*)d_in[3];
  const u16* w1 = (const u16*)d_in[5]; const u16* w2 = (const u16*)d_in[6];
  u16* out = (u16*)d_out;
  char* ws = (char*)d_ws;
  u16* xn = (u16*)ws; ws += (size_t)S * D * 2;
  u16* qb = (u16*)ws; ws += (size_t)S * D * 2;
  u16* hbuf = (u16*)ws; ws += (size_t)S * F * 2;
  float* kvslab = (float*)ws; ws += 16 * 32 * 720 * 4;
  float* kv = (float*)ws; ws += 32 * 720 * 4;
  u16* o = (u16*)ws; ws += (size_t)S * D * 2;
  float* slab = (float*)ws; ws += (size_t)4 * S * D * 4;
  u16* wf = (u16*)ws; ws += (size_t)D * 2 * D * 2;
  k_ln<<<S / 4 + 144, 256, 0, stream>>>(x, xn, wqkv, wphi, wf);
  k_l1<<<256, 512, 0, stream>>>(xn, w1, wqkv, wf, hbuf, qb, kvslab);
  k_qo3<<<256, 256, 0, stream>>>(qb, kvslab, o);
  k_g2<<<256, 512, 0, stream>>>(hbuf, w2, o, wo, slab);
  k_l3<<<S * D / 1024, 256, 0, stream>>>(slab, x, out);
}
```

```cpp
#include <hip/hip_runtime.h>
#include <hip/hip_bf16.h>
#include <stdint.h>

typedef __attribute__((ext_vector_type(8))) short bf16x8;
typedef __attribute__((ext_vector_type(4))) short s16x4;
typedef __attribute__((ext_vector_type(4))) float f32x4;
typedef unsigned short u16;
#define LDS_AS __attribute__((address_space(3)))

__device__ __forceinline__ float bf2f(u16 v) { return __uint_as_float(((unsigned)v) << 16); }
__device__ __forceinline__ u16 f2bf(float f) {
  unsigned u = __float_as_uint(f);
  u += 0x7fffu + ((u >> 16) & 1u);
  return (u16)(u >> 16);
}
__device__ __forceinline__ unsigned pack2(float lo, float hi) { unsigned r; asm("v_cvt_pk_bf16_f32 %0, %1, %2" : "=v"(r) : "v"(lo), "v"(hi)); return r; }
__device__ __forceinline__ float rbf(float f) { return __uint_as_float(pack2(f, f) << 16); }

constexpr int S = 2048, D = 576, H = 32, DH = 18, F = 4608;

#define STAMPK(k, i)
__global__ __launch_bounds__(256) void k_ln(const u16* __restrict__ x, u16* __restrict__ xn, const u16* __restrict__ wqkv, const u16* __restrict__ wphi, u16* __restrict__ wf) {
  if (blockIdx.x >= S / 4) {
    int u = __builtin_amdgcn_readfirstlane((blockIdx.x - S / 4) * 4 + (threadIdx.x >> 6));
    int wh = u & 63, k = (u >> 6) * 64 + (threadIdx.x & 63), h = wh & 31;
    const u16* src = wqkv + (size_t)k * (3 * D) + wh * 18;
    const unsigned* W = (const unsigned*)(wphi + h * 324);
    float a[18];
#pragma unroll
    for (int i = 0; i < 9; ++i) { unsigned v = *(const unsigned*)(src + 2 * i); a[2 * i] = __uint_as_float(v << 16); a[2 * i + 1] = __uint_as_float(v & 0xffff0000u); }
    u16* dst = wf + (size_t)k * (2 * D) + wh * 18;
#pragma unroll
    for (int f = 0; f < 18; f += 2) {
      float s0 = 0.f, s1 = 0.f;
#pragma unroll
      for (int d = 0; d < 18; ++d) { unsigned w = W[d * 9 + (f >> 1)]; s0 = fmaf(a[d], __uint_as_float(w << 16), s0); s1 = fmaf(a[d], __uint_as_float(w & 0xffff0000u), s1); }
      *(unsigned*)(dst + f) = pack2(s0, s1);
    }
    return;
  }
  int row = blockIdx.x * 4 + (threadIdx.x >> 6), lane = threadIdx.x & 63;
  const u16* xr = x + (size_t)row * D;
  float v[9]; float s = 0.f;
  for (int i = 0; i < 9; ++i) { v[i] = bf2f(xr[lane + 64 * i]); s += v[i]; }
  for (int o = 32; o; o >>= 1) s += __shfl_xor(s, o);
  float m = s * (1.f / D);
  float q = 0.f;
  for (int i = 0; i < 9; ++i) { float d = v[i] - m; q += d * d; }
  for (int o = 32; o; o >>= 1) q += __shfl_xor(q, o);
  float r = rsqrtf(q * (1.f / D) + 1e-5f);
  for (int i = 0; i < 9; ++i) xn[(size_t)row * D + lane + 64 * i] = f2bf((v[i] - m) * r);
}

__device__ __forceinline__ void glds16_asm(const void* gsrc, unsigned lds_dst) {
  unsigned keep;
  asm volatile("s_mov_b32 %0, m0\n\ts_mov_b32 m0, %2\n\ts_nop 0\n\tglobal_load_lds_dwordx4 %1, off\n\ts_mov_b32 m0, %0" : "=&s"(keep) : "v"(gsrc), "s"(lds_dst) : "memory");
}
#define GLDS(g, l) glds16_asm((const void*)(g), (unsigned)(size_t)(l))
#define WAIT_V(n) asm volatile("s_waitcnt vmcnt(" #n ")" ::: "memory")
#define WAIT_L0() asm volatile("s_waitcnt lgkmcnt(0)" ::: "memory")
#define BAR() __builtin_amdgcn_s_barrier()

__device__ __forceinline__ int swap23(int r) { return (r & ~0xC) | ((r & 4) << 1) | ((r & 8) >> 1); }

template <int N> __device__ __forceinline__ void wait_v() { asm volatile("s_waitcnt vmcnt(%0)" :: "n"(N) : "memory"); }
template <int MT, int NTW, int NCH, int NS>
struct Core {
  static constexpr int PITCH = NCH * 16;
  static constexpr int NB = (32 * NCH + 63) / 64;
  static constexpr int STAGE = 8192 + NB * 1024;
  static constexpr int NLO = 1 + NB / 8, NHI = 1 + (NB + 7) / 8, XW = NB % 8;
  template <int K> static __device__ __forceinline__ void wait_stages(int wid) {
    if (XW == 0) wait_v<K * NLO>();
    else { if (wid < XW) wait_v<K * NHI>(); else wait_v<K * NLO>(); }
  }
};

template <int MT, int NTW, int NCH, int NS, int KID, class BSRC>
__device__ __forceinline__ void gemm_core(int NIT, char* smem, const u16* ag0, BSRC&& bsrc, f32x4 (&acc)[MT][NTW], int a_row0  , int tile0  ,
                                          int nsw, const u16* ag1) {
  using C = Core<MT, NTW, NCH, NS>;
  const int tid = threadIdx.x, lane = tid & 63;
  const int wid = __builtin_amdgcn_readfirstlane(tid >> 6);
  const int fr = lane & 15, fq = lane >> 4;
  const u16* ag = ag0;
  const u16* bg[4]; int bst[4];
#pragma unroll
  for (int i = 0; i < 4; ++i) {
    int ci = (wid + 8 * i) * 64 + lane;
    int rho = ci / NCH, c = ci - rho * NCH;
    if (rho > 31) { rho = 31; }
    bsrc(swap23(rho), c, bg[i], bst[i], 0);
  }
  int nissued = 0;
  auto issue = [&](int stg) {
    if (nissued == nsw) {
      ag = ag1;
#pragma unroll
      for (int i = 0; i < 4; ++i) {
        int ci = (wid + 8 * i) * 64 + lane;
        int rho = ci / NCH, c = ci - rho * NCH;
        if (rho > 31) { rho = 31; }
        bsrc(swap23(rho), c, bg[i], bst[i], 1);
      }
    }
    ++nissued;
    char* sb = smem + (stg % NS) * C::STAGE;
    GLDS(ag, sb + wid * 1024); ag += 32;
#pragma unroll
    for (int i = 0; i < 4; ++i) {
      if (8 * i < C::NB && (8 * i + 8 <= C::NB || wid < C::NB - 8 * i)) GLDS(bg[i], sb + 8192 + (wid + 8 * i) * 1024);
      bg[i] += bst[i];
    }
  };
  const int a_base = (a_row0 + fr) * 64 + ((fq ^ ((-(fr >> 2)) & 3)) * 16);
  const int b_base = 8192 + (16 * (fq >> 1) + 4 * (fq & 1) + (fr >> 2)) * C::PITCH + (2 * tile0 + ((lane & 3) >> 1)) * 16 + 8 * (lane & 1);
  bf16x8 afA[MT], bfA[NTW], afB[MT], bfB[NTW];
  auto ldfr = [&](int stg, bf16x8 (&af)[MT], bf16x8 (&bf)[NTW]) {
    typedef LDS_AS s16x4* lp;
    const char* nb = smem + (stg % NS) * C::STAGE;
#pragma unroll
    for (int j = 0; j < NTW; ++j) {
      s16x4 lo = __builtin_amdgcn_ds_read_tr16_b64_v4i16((lp)(nb + b_base + j * 32));
      s16x4 hi = __builtin_amdgcn_ds_read_tr16_b64_v4i16((lp)(nb + b_base + j * 32 + 8 * C::PITCH));
      bf[j] = __builtin_shufflevector(lo, hi, 0, 1, 2, 3, 4, 5, 6, 7);
    }
#pragma unroll
    for (int m = 0; m < MT; ++m) af[m] = *(const bf16x8*)(nb + a_base + m * 1024);
  };
  auto mma = [&](bf16x8 (&af)[MT], bf16x8 (&bf)[NTW]) {
#pragma unroll
    for (int j = 0; j < NTW; ++j)
#pragma unroll
      for (int m = 0; m < MT; ++m)
        acc[m][j] = __builtin_amdgcn_mfma_f32_16x16x32_bf16(bf[j], af[m], acc[m][j], 0, 0, 0);
  };
  constexpr int NRD = 2 * NTW + MT, NMM = MT * NTW;
  constexpr int MPR = NMM / NRD > 0 ? NMM / NRD : 1;
#define CORE_IL() do { _Pragma("unroll") for (int _i = 0; _i < NRD; ++_i) { __builtin_amdgcn_sched_group_barrier(0x100, 1, 0); __builtin_amdgcn_sched_group_barrier(0x8, MPR, 0); } __builtin_amdgcn_sched_group_barrier(0x8, 64, 0); } while (0)
  STAMPK(KID, 0);
#pragma unroll
  for (int s = 0; s < NS - 1; ++s) issue(s);
  C::template wait_stages<NS - 2>(wid);
  BAR();
  STAMPK(KID, 1);
  ldfr(0, afA, bfA);
#pragma unroll 1
  for (int t = 0; t + NS < NIT; t += 2) {
    C::template wait_stages<NS - 3>(wid); BAR();
    issue(t + NS - 1);
    ldfr(t + 1, afB, bfB); mma(afA, bfA); CORE_IL();
    C::template wait_stages<NS - 3>(wid); BAR();
    issue(t + NS);
    ldfr(t + 2, afA, bfA); mma(afB, bfB); CORE_IL();
  }
  static_assert(NS == 4 || NS == 8, "NS");
  C::template wait_stages<NS - 3>(wid); BAR();
  issue(NIT - 1);
  ldfr(NIT - NS + 1, afB, bfB); mma(afA, bfA); CORE_IL();
  if (NS == 8) {
    C::template wait_stages<5>(wid); BAR(); ldfr(NIT - 6, afA, bfA); mma(afB, bfB); CORE_IL();
    C::template wait_stages<4>(wid); BAR(); ldfr(NIT - 5, afB, bfB); mma(afA, bfA); CORE_IL();
    C::template wait_stages<3>(wid); BAR(); ldfr(NIT - 4, afA, bfA); mma(afB, bfB); CORE_IL();
    C::template wait_stages<2>(wid); BAR(); ldfr(NIT - 3, afB, bfB); mma(afA, bfA); CORE_IL();
  }
  C::template wait_stages<1>(wid); BAR(); ldfr(NIT - 2, afA, bfA); mma(afB, bfB); CORE_IL();
  C::template wait_stages<0>(wid); BAR(); ldfr(NIT - 1, afB, bfB); mma(afA, bfA); CORE_IL();
  mma(afB, bfB);
  STAMPK(KID, 2);
}

#define SCHED_IL() do { _Pragma("unroll") for (int _i = 0; _i < 9; ++_i) { __builtin_amdgcn_sched_group_barrier(0x100, 2, 0); __builtin_amdgcn_sched_group_barrier(0x8, 1, 0); } __builtin_amdgcn_sched_group_barrier(0x8, 32, 0); } while (0)

__device__ __forceinline__ bf16x8 tr_frag(const char* p) {
  typedef LDS_AS s16x4* lp;
  s16x4 lo = __builtin_amdgcn_ds_read_tr16_b64_v4i16((lp)(p));
  s16x4 hi = __builtin_amdgcn_ds_read_tr16_b64_v4i16((lp)(p + 4096));
  return __builtin_shufflevector(lo, hi, 0, 1, 2, 3, 4, 5, 6, 7);
}

template <int TYPE>
__device__ __forceinline__ void l1_wg(char* smem, const u16* __restrict__ xn, const u16* __restrict__ w1, const u16* __restrict__ wqkv,
                                      const u16* __restrict__ wf, u16* __restrict__ hbuf, u16* __restrict__ qb, float* __restrict__ kvslab,
                                      int rt, int ct) {
  constexpr int NCH = TYPE ? 38 : 54;
  constexpr int NTW = TYPE ? 5 : 7;
  const int tid = threadIdx.x, lane = tid & 63;
  const int wid = __builtin_amdgcn_readfirstlane(tid >> 6);
  const int wr = wid >> 2, wc = wid & 3, fr = lane & 15, fq = lane >> 4;
  const u16* ag = xn + (size_t)(rt * 128 + wid * 16 + (lane >> 2)) * D + (((lane & 3) ^ ((-(lane >> 4)) & 3)) * 8);
  f32x4 acc[4][NTW];
#pragma unroll
  for (int m = 0; m < 4; ++m)
#pragma unroll
    for (int j = 0; j < NTW; ++j) acc[m][j] = f32x4{0.f, 0.f, 0.f, 0.f};
  auto bsrc = [&](int k, int c, const u16*& p, int& st, int phase) {
    if (TYPE == 0) {
      int gc = ct * 432 + c * 8;
      if (gc < F) { p = w1 + (size_t)k * F + gc; st = 32 * F; }
      else { p = wf + (size_t)k * (2 * D) + (gc - F); st = 32 * 2 * D; }
    } else {
      if (c > 35) c = 35;
      int part = c >= 18 ? 1 : 0, cc = c - 18 * part;
      if (part == 0) { p = wf + (size_t)k * (2 * D) + D + ct * 144 + cc * 8; st = 32 * 2 * D; }
      else { p = wqkv + (size_t)k * (3 * D) + 2 * D + ct * 144 + cc * 8; st = 32 * 3 * D; }
    }
  };
  gemm_core<4, NTW, NCH, 4, 1>(18, smem, ag, bsrc, acc, wr * 64, NTW * wc, 1 << 30, nullptr);
  if (TYPE == 0) {
#pragma unroll
    for (int m = 0; m < 4; ++m)
#pragma unroll
      for (int j = 0; j < NTW; ++j) {
        int T = NTW * wc + j;
        if (T < 27) {
          int row = rt * 128 + wr * 64 + m * 16 + fr;
          int col0 = ct * 432 + T * 16 + fq * 4;
          f32x4 v = acc[m][j];
          if (col0 < F) {
            float o[4];
#pragma unroll
            for (int e = 0; e < 4; ++e) o[e] = v[e] * __builtin_amdgcn_rcpf(1.f + __builtin_amdgcn_exp2f(v[e] * -1.44269504f));
            uint2 pk = make_uint2(pack2(o[0], o[1]), pack2(o[2], o[3]));
            *(uint2*)(hbuf + (size_t)row * F + col0) = pk;
          } else {
            uint2 pk = make_uint2(pack2(v[0], v[1]), pack2(v[2], v[3]));
            *(uint2*)(qb + (size_t)row * D + (col0 - F)) = pk;
          }
        }
      }
  } else {
    constexpr int PK_PITCH = 608, VT_PITCH = 352;
    char* Pk = smem;
    char* Vt = smem + 128 * PK_PITCH;
    __syncthreads();
#pragma unroll
    for (int m = 0; m < 4; ++m)
#pragma unroll
      for (int j = 0; j < NTW; ++j) {
        int T = NTW * wc + j;
        if (T < 18) {
          int row = wr * 64 + m * 16 + fr;
#pragma unroll
          for (int e = 0; e < 4; ++e) {
            int c = T * 16 + fq * 4 + e;
            int part = c >= 144 ? 1 : 0, cc = c - 144 * part, hd = cc / 18, d = cc - hd * 18;
            u16 val = f2bf(acc[m][j][e]);
            if (part == 0) *(u16*)(Pk + row * PK_PITCH + (hd * 36 + d) * 2) = val;
            else *(u16*)(Vt + row * VT_PITCH + (hd * 20 + d) * 2) = val;
          }
        }
      }
    for (int i = tid; i < 128 * 8; i += 512) { int row = i >> 3, hd = i & 7; *(u16*)(Vt + row * VT_PITCH + (hd * 20 + 18) * 2) = 0x3f80; }
    __syncthreads();
    {
      {
        unsigned* pr0 = (unsigned*)(Pk + lane * PK_PITCH + wid * 72);
        unsigned* pr1 = (unsigned*)(Pk + (lane + 64) * PK_PITCH + wid * 72);
        float p0[18], p1[18];
#pragma unroll
        for (int i = 0; i < 9; ++i) {
          unsigned u = pr0[i]; p0[2 * i] = __uint_as_float(u << 16); p0[2 * i + 1] = __uint_as_float(u & 0xffff0000u);
          unsigned w = pr1[i]; p1[2 * i] = __uint_as_float(w << 16); p1[2 * i + 1] = __uint_as_float(w & 0xffff0000u);
        }
        auto finish = [&](float* p, unsigned* pr) {
          float mx = -1e30f, mn = 1e30f;
#pragma unroll
          for (int f = 0; f < 18; ++f) { mx = fmaxf(mx, p[f]); mn = fminf(mn, p[f]); }
          float e1[18], e2[18], s1 = 0.f, s2 = 0.f;
#pragma unroll
          for (int f = 0; f < 18; ++f) { e1[f] = __expf(p[f] - mx); s1 += e1[f]; e2[f] = __expf(mn - p[f]); s2 += e2[f]; }
          float r1 = 1.f / s1, r2 = 1.f / s2;
#pragma unroll
          for (int i = 0; i < 9; ++i) { pr[i] = pack2(e1[2 * i] * r1, e1[2 * i + 1] * r1); pr[9 + i] = pack2(e2[2 * i] * r2, e2[2 * i + 1] * r2); }
        };
        finish(p0, pr0);
        finish(p1, pr1);
      }
    }
    __syncthreads();
    {
      typedef LDS_AS s16x4* lp;
      f32x4 kvacc[3][2];
#pragma unroll
      for (int a = 0; a < 3; ++a) for (int b = 0; b < 2; ++b) kvacc[a][b] = f32x4{0.f, 0.f, 0.f, 0.f};
      const int q = fr >> 2, p = lane & 3;
#pragma unroll
      for (int ks = 0; ks < 4; ++ks) {
        bf16x8 pa[3], vb[2];
        int r0 = ks * 32 + 8 * fq + q;
#pragma unroll
        for (int mt = 0; mt < 3; ++mt) {
          const char* a0 = Pk + r0 * PK_PITCH + (wid * 36 + mt * 16 + 4 * p) * 2;
          s16x4 lo = __builtin_amdgcn_ds_read_tr16_b64_v4i16((lp)(a0));
          s16x4 hi = __builtin_amdgcn_ds_read_tr16_b64_v4i16((lp)(a0 + 4 * PK_PITCH));
          pa[mt] = __builtin_shufflevector(lo, hi, 0, 1, 2, 3, 4, 5, 6, 7);
        }
#pragma unroll
        for (int nt = 0; nt < 2; ++nt) {
          const char* b0 = Vt + r0 * VT_PITCH + (wid * 20 + nt * 16 + 4 * p) * 2;
          s16x4 lo = __builtin_amdgcn_ds_read_tr16_b64_v4i16((lp)(b0));
          s16x4 hi = __builtin_amdgcn_ds_read_tr16_b64_v4i16((lp)(b0 + 4 * VT_PITCH));
          vb[nt] = __builtin_shufflevector(lo, hi, 0, 1, 2, 3, 4, 5, 6, 7);
        }
#pragma unroll
        for (int mt = 0; mt < 3; ++mt)
#pragma unroll
          for (int nt = 0; nt < 2; ++nt) kvacc[mt][nt] = __builtin_amdgcn_mfma_f32_16x16x32_bf16(pa[mt], vb[nt], kvacc[mt][nt], 0, 0, 0);
      }
      float* dst = kvslab + ((size_t)rt * 32 + ct * 8 + wid) * 720;
#pragma unroll
      for (int mt = 0; mt < 3; ++mt)
#pragma unroll
        for (int nt = 0; nt < 2; ++nt)
#pragma unroll
          for (int e = 0; e < 4; ++e) {
            int f = mt * 16 + fq * 4 + e, d = nt * 16 + fr;
            if (f < 36 && d < 19) dst[f * 20 + d] = kvacc[mt][nt][e];
          }
    }
  }
}

__global__ __launch_bounds__(512, 2) void k_l1(const u16* __restrict__ xn, const u16* __restrict__ w1, const u16* __restrict__ wqkv,
                                               const u16* __restrict__ wf, u16* __restrict__ hbuf, u16* __restrict__ qb, float* __restrict__ kvslab) {
  __shared__ __attribute__((aligned(16))) char smem[163840];
  int bid = blockIdx.x, xcd = bid & 7, idx = bid >> 3;
  int rt = (xcd & 1) * 8 + (idx & 7);
  int cs = (xcd >> 1) * 4 + (idx >> 3);
  if (cs < 12) l1_wg<0>(smem, xn, w1, wqkv, wf, hbuf, qb, kvslab, rt, cs);
  else l1_wg<1>(smem, xn, w1, wqkv, wf, hbuf, qb, kvslab, rt, cs - 12);
  STAMPK(1, 3);
}

constexpr int G3_A = 32768, G3_B = 36864, G3_STAGE = G3_A + G3_B;
__global__ __launch_bounds__(512, 2) void k_g2(const u16* __restrict__ hbuf, const u16* __restrict__ w2, const u16* __restrict__ o, const u16* __restrict__ wo,
                                               float* __restrict__ slab) {
  __shared__ __attribute__((aligned(16))) char smem[147456];
  int bid = blockIdx.x, xcd = bid & 7, idx = bid >> 3;
  int ks = xcd >> 1, rt = (xcd & 1) * 8 + (idx & 7), ct = idx >> 3;
  const int tid = threadIdx.x, lane = tid & 63, fr = lane & 15, fq = lane >> 4;
  const int wid = __builtin_amdgcn_readfirstlane(tid >> 6), wm = wid & 1, g = wid >> 1;
  f32x4 acc[4][9];
#pragma unroll
  for (int m = 0; m < 4; ++m)
#pragma unroll
    for (int j = 0; j < 9; ++j) acc[m][j] = f32x4{0.f, 0.f, 0.f, 0.f};
  const int nst = ks == 3 ? 11 : 10, nsw = ks == 3 ? 6 : 100;
  const u16* ap[4]; const u16* bp[5];
  STAMPK(2, 0);
#pragma unroll
  for (int i = 0; i < 4; ++i) {
    int row = (wid * 4 + i) * 4 + (lane >> 4), lc = (lane & 15) ^ (row & 15);
    ap[i] = hbuf + (size_t)(rt * 128 + row) * F + ks * 1280 + lc * 8;
  }
#pragma unroll
  for (int i = 0; i < 5; ++i) {
    int ci = (wid + 8 * i) * 64 + lane; if (ci > 2303) ci = 2303;
    int rp = ci / 18, c = ci - rp * 18, k = (rp & ~31) | swap23(rp & 31);
    bp[i] = w2 + (size_t)(ks * 1280 + k) * D + ct * 144 + c * 8;
  }
  auto issue = [&](int t) {
    if (t == nsw) {
#pragma unroll
      for (int i = 0; i < 4; ++i) {
        int row = (wid * 4 + i) * 4 + (lane >> 4), lc = (lane & 15) ^ (row & 15);
        ap[i] = o + (size_t)(rt * 128 + row) * D + lc * 8;
      }
#pragma unroll
      for (int i = 0; i < 5; ++i) {
        int ci = (wid + 8 * i) * 64 + lane; if (ci > 2303) ci = 2303;
        int rp = ci / 18, c = ci - rp * 18, k = (rp & ~31) | swap23(rp & 31);
        bp[i] = wo + (size_t)k * D + ct * 144 + c * 8;
      }
    }
    char* sb = smem + (t & 1) * G3_STAGE;
    if (t == 10) {
#pragma unroll
      for (int i = 0; i < 4; ++i) { int row = (wid * 4 + i) * 4 + (lane >> 4); if ((((lane & 15) ^ (row & 15)) & 8) != 0) ap[i] -= 64; }
#pragma unroll
      for (int i = 0; i < 5; ++i) { int ci = (wid + 8 * i) * 64 + lane; if (ci > 2303) ci = 2303; if (ci >= 1152) bp[i] -= (size_t)64 * D; }
    }
#pragma unroll
    for (int i = 0; i < 4; ++i) { GLDS(ap[i], sb + (wid * 4 + i) * 1024); ap[i] += 128; }
#pragma unroll
    for (int i = 0; i < 5; ++i) { if (i < 4 || wid < 4) GLDS(bp[i], sb + G3_A + (wid + 8 * i) * 1024); bp[i] += (size_t)128 * D; }
  };
  const int a_base = (wm * 64 + fr) * 256 + (((g * 4 + fq) ^ fr) * 16);
  const int b_base = G3_A + (g * 32 + 16 * (fq >> 1) + 4 * (fq & 1) + (fr >> 2)) * 288 + ((lane & 3) >> 1) * 16 + 8 * (lane & 1);
  issue(0);
#pragma unroll 1
  for (int t = 0; t < nst; ++t) {
    WAIT_V(0); BAR();
    if (t == 0) STAMPK(2, 1);
    if (t + 1 < nst) issue(t + 1);
    if (t == 10 && g >= 2) break;
    typedef LDS_AS s16x4* lp;
    const char* nb = smem + (t & 1) * G3_STAGE;
    bf16x8 af[4], bf[9];
#pragma unroll
    for (int m = 0; m < 4; ++m) af[m] = *(const bf16x8*)(nb + a_base + m * 4096);
#pragma unroll
    for (int j = 0; j < 9; ++j) {
      s16x4 lo = __builtin_amdgcn_ds_read_tr16_b64_v4i16((lp)(nb + b_base + j * 32));
      s16x4 hi = __builtin_amdgcn_ds_read_tr16_b64_v4i16((lp)(nb + b_base + j * 32 + 8 * 288));
      bf[j] = __builtin_shufflevector(lo, hi, 0, 1, 2, 3, 4, 5, 6, 7);
    }
#pragma unroll
    for (int j = 0; j < 9; ++j)
#pragma unroll
      for (int m = 0; m < 4; ++m) acc[m][j] = __builtin_amdgcn_mfma_f32_16x16x32_bf16(bf[j], af[m], acc[m][j], 0, 0, 0);
    __builtin_amdgcn_sched_group_barrier(0x100, 8, 0);
#pragma unroll
    for (int j = 0; j < 7; ++j) { __builtin_amdgcn_sched_group_barrier(0x8, 4, 0); __builtin_amdgcn_sched_group_barrier(0x100, 2, 0); }
    __builtin_amdgcn_sched_group_barrier(0x8, 8, 0);
  }
  STAMPK(2, 2);
  __syncthreads();
  f32x4* red = (f32x4*)smem;
  if (g >= 2) {
#pragma unroll
    for (int m = 0; m < 4; ++m)
#pragma unroll
      for (int j = 0; j < 9; ++j) red[(((g - 2) * 2 + wm) * 36 + m * 9 + j) * 64 + lane] = acc[m][j];
  }
  __syncthreads();
  if (g < 2) {
#pragma unroll
    for (int m = 0; m < 4; ++m)
#pragma unroll
      for (int j = 0; j < 9; ++j) acc[m][j] += red[((g * 2 + wm) * 36 + m * 9 + j) * 64 + lane];
  }
  __syncthreads();
  if (g == 1) {
#pragma unroll
    for (int m = 0; m < 4; ++m)
#pragma unroll
      for (int j = 0; j < 9; ++j) red[(wm * 36 + m * 9 + j) * 64 + lane] = acc[m][j];
  }
  __syncthreads();
  if (g == 0) {
    float* dst = slab + (size_t)ks * S * D;
#pragma unroll
    for (int m = 0; m < 4; ++m)
#pragma unroll
      for (int j = 0; j < 9; ++j)
        *(f32x4*)(dst + (size_t)(rt * 128 + wm * 64 + m * 16 + fr) * D + ct * 144 + j * 16 + fq * 4) = acc[m][j] + red[(wm * 36 + m * 9 + j) * 64 + lane];
  }
  STAMPK(2, 3);
}

__global__ __launch_bounds__(256) void k_l3(const float* __restrict__ slab, const u16* __restrict__ x, u16* __restrict__ out) {
  int i = (blockIdx.x * 256 + threadIdx.x) * 4;
  f32x4 v = *(const f32x4*)(slab + i);
#pragma unroll
  for (int k = 1; k < 4; ++k) v += *(const f32x4*)(slab + (size_t)k * S * D + i);
  uint2 xv = *(const uint2*)(x + i);
  float x0 = __uint_as_float(xv.x << 16), x1 = __uint_as_float(xv.x & 0xffff0000u), x2 = __uint_as_float(xv.y << 16), x3 = __uint_as_float(xv.y & 0xffff0000u);
  *(uint2*)(out + i) = make_uint2(pack2(v[0] + x0, v[1] + x1), pack2(v[2] + x2, v[3] + x3));
}

__global__ __launch_bounds__(256) void k_qo3(const u16* __restrict__ pq, const float* __restrict__ kvslab, u16* __restrict__ o) {
  __shared__ __attribute__((aligned(16))) u16 kvT[32 * 72];
  const int h = blockIdx.x >> 3, rg = blockIdx.x & 7, tid = threadIdx.x, lane = tid & 63, wid = tid >> 6;
  const int fr = lane & 15, fq = lane >> 4;
  const int base = fq * 5 - (fq == 3 ? 1 : 0), cnt = fq < 2 ? 5 : 4;
  float p[4][5];
#pragma unroll
  for (int mt = 0; mt < 4; ++mt) {
    const u16* pr = pq + (size_t)(rg * 256 + wid * 64 + mt * 16 + fr) * D + h * DH + base;
#pragma unroll
    for (int j = 0; j < 5; ++j) p[mt][j] = bf2f(pr[j < cnt ? j : 0]);
  }
  {
    float v[3][16];
#pragma unroll
    for (int i = 0; i < 3; ++i) {
      int e = tid + 256 * i; if (e > 683) e = 683;
      int f = e / 19, n = e - f * 19;
#pragma unroll
      for (int r = 0; r < 16; ++r) v[i][r] = kvslab[((size_t)r * 32 + h) * 720 + f * 20 + n];
    }
    for (int i = tid; i < 32 * 36; i += 256) ((unsigned*)kvT)[i] = 0u;
    __syncthreads();
#pragma unroll
    for (int i = 0; i < 3; ++i) {
      int e = tid + 256 * i;
      float t = 0.f;
#pragma unroll
      for (int r = 0; r < 16; ++r) t += v[i][r];
      if (e < 684) {
        int f = e / 19, n = e - f * 19;
        int s = f >= 18 ? 1 : 0, d = f - 18 * s;
        int q = d < 10 ? d / 5 : 2 + (d - 10) / 4;
        int j = d - (q * 5 - (q == 3 ? 1 : 0));
        kvT[n * 72 + s * 32 + q * 8 + j] = f2bf(t);
      }
    }
  }
  __syncthreads();
  bf16x8 bfr[2][2];
#pragma unroll
  for (int s = 0; s < 2; ++s)
#pragma unroll
    for (int j = 0; j < 2; ++j) bfr[s][j] = *(const bf16x8*)(kvT + (j * 16 + fr) * 72 + s * 32 + fq * 8);
#pragma unroll
  for (int mt = 0; mt < 4; ++mt) {
    float mx = p[mt][0], mn = p[mt][0];
#pragma unroll
    for (int j = 1; j < 5; ++j) { mx = fmaxf(mx, p[mt][j]); mn = fminf(mn, p[mt][j]); }
    mx = fmaxf(mx, __shfl_xor(mx, 16)); mx = fmaxf(mx, __shfl_xor(mx, 32));
    mn = fminf(mn, __shfl_xor(mn, 16)); mn = fminf(mn, __shfl_xor(mn, 32));
    float e1[5], e2[5], s1 = 0.f, s2 = 0.f;
#pragma unroll
    for (int j = 0; j < 5; ++j) {
      e1[j] = j < cnt ? __expf(p[mt][j] - mx) : 0.f; e2[j] = j < cnt ? __expf(mn - p[mt][j]) : 0.f;
      s1 += e1[j]; s2 += e2[j];
    }
    s1 += __shfl_xor(s1, 16); s1 += __shfl_xor(s1, 32);
    s2 += __shfl_xor(s2, 16); s2 += __shfl_xor(s2, 32);
    float r1 = 1.f / s1, r2 = 1.f / s2;
    typedef __attribute__((ext_vector_type(4))) unsigned u32x4;
    u32x4 a0 = {pack2(e1[0] * r1, e1[1] * r1), pack2(e1[2] * r1, e1[3] * r1), pack2(e1[4] * r1, 0.f), 0u};
    u32x4 a1 = {pack2(e2[0] * r2, e2[1] * r2), pack2(e2[2] * r2, e2[3] * r2), pack2(e2[4] * r2, 0.f), 0u};
    bf16x8 af0 = __builtin_bit_cast(bf16x8, a0), af1 = __builtin_bit_cast(bf16x8, a1);
    f32x4 acc[2];
#pragma unroll
    for (int j = 0; j < 2; ++j) {
      acc[j] = f32x4{0.f, 0.f, 0.f, 0.f};
      acc[j] = __builtin_amdgcn_mfma_f32_16x16x32_bf16(bfr[0][j], af0, acc[j], 0, 0, 0);
      acc[j] = __builtin_amdgcn_mfma_f32_16x16x32_bf16(bfr[1][j], af1, acc[j], 0, 0, 0);
    }
    float den = rbf(__shfl(acc[1][2], fr)) + 1e-6f;
    float rd = 1.f / den;
    unsigned* dst = (unsigned*)(o + (size_t)(rg * 256 + wid * 64 + mt * 16 + fr) * D + h * DH);
    dst[2 * fq] = pack2(rbf(acc[0][0]) * rd, rbf(acc[0][1]) * rd);
    dst[2 * fq + 1] = pack2(rbf(acc[0][2]) * rd, rbf(acc[0][3]) * rd);
    if (fq == 0) dst[8] = pack2(rbf(acc[1][0]) * rd, rbf(acc[1][1]) * rd);
  }
}

extern "C" void kernel_launch(void* const* d_in, const int* in_sizes, int n_in, void* d_out, int out_size, void* d_ws, size_t ws_size, hipStream_t stream) {
  const u16* x = (const u16*)d_in[0]; const u16* wqkv = (const u16*)d_in[1]; const u16* wphi = (const u16*)d_in[2]; const u16* wo = (const u16*)d_in[3];
  const u16* w1 = (const u16*)d_in[5]; const u16* w2 = (const u16*)d_in[6];
  u16* out = (u16*)d_out;
  char* ws = (char*)d_ws;
  u16* xn = (u16*)ws; ws += (size_t)S * D * 2;
  u16* qb = (u16*)ws; ws += (size_t)S * D * 2;
  u16* hbuf = (u16*)ws; ws += (size_t)S * F * 2;
  float* kvslab = (float*)ws; ws += 16 * 32 * 720 * 4;
  float* kv = (float*)ws; ws += 32 * 720 * 4;
  u16* o = (u16*)ws; ws += (size_t)S * D * 2;
  float* slab = (float*)ws; ws += (size_t)4 * S * D * 4;
  u16* wf = (u16*)ws; ws += (size_t)D * 2 * D * 2;
  k_ln<<<S / 4 + 144, 256, 0, stream>>>(x, xn, wqkv, wphi, wf);
  k_l1<<<256, 512, 0, stream>>>(xn, w1, wqkv, wf, hbuf, qb, kvslab);
  k_qo3<<<256, 256, 0, stream>>>(qb, kvslab, o);
  k_g2<<<256, 512, 0, stream>>>(hbuf, w2, o, wo, slab);
  k_l3<<<S * D / 1024, 256, 0, stream>>>(slab, x, out);
}
```

```cpp
#include <hip/hip_runtime.h>
#include <hip/hip_bf16.h>
#include <stdint.h>

typedef __attribute__((ext_vector_type(8))) short bf16x8;
typedef __attribute__((ext_vector_type(4))) short s16x4;
typedef __attribute__((ext_vector_type(4))) float f32x4;
typedef unsigned short u16;
#define LDS_AS __attribute__((address_space(3)))

__device__ __forceinline__ float bf2f(u16 v) { return __uint_as_float(((unsigned)v) << 16); }
__device__ __forceinline__ u16 f2bf(float f) {
  unsigned u = __float_as_uint(f);
  u += 0x7fffu + ((u >> 16) & 1u);
  return (u16)(u >> 16);
}
__device__ __forceinline__ unsigned pack2(float lo, float hi) { unsigned r; asm("v_cvt_pk_bf16_f32 %0, %1, %2" : "=v"(r) : "v"(lo), "v"(hi)); return r; }
__device__ __forceinline__ float rbf(float f) { return __uint_as_float(pack2(f, f) << 16); }

constexpr int S = 2048, D = 576, H = 32, DH = 18, F = 4608;

#define STAMPK(k, i)
__global__ __launch_bounds__(256) void k_ln(const u16* __restrict__ x, u16* __restrict__ xn, const u16* __restrict__ wqkv, const u16* __restrict__ wphi, u16* __restrict__ wf) {
  if (blockIdx.x >= S / 4) {
    int u = __builtin_amdgcn_readfirstlane((blockIdx.x - S / 4) * 4 + (threadIdx.x >> 6));
    int wh = u & 63, k = (u >> 6) * 64 + (threadIdx.x & 63), h = wh & 31;
    const u16* src = wqkv + (size_t)k * (3 * D) + wh * 18;
    const unsigned* W = (const unsigned*)(wphi + h * 324);
    float a[18];
#pragma unroll
    for (int i = 0; i < 9; ++i) { unsigned v = *(const unsigned*)(src + 2 * i); a[2 * i] = __uint_as_float(v << 16); a[2 * i + 1] = __uint_as_float(v & 0xffff0000u); }
    u16* dst = wf + (size_t)k * (2 * D) + wh * 18;
#pragma unroll
    for (int f = 0; f < 18; f += 2) {
      float s0 = 0.f, s1 = 0.f;
#pragma unroll
      for (int d = 0; d < 18; ++d) { unsigned w = W[d * 9 + (f >> 1)]; s0 = fmaf(a[d], __uint_as_float(w << 16), s0); s1 = fmaf(a[d], __uint_as_float(w & 0xffff0000u), s1); }
      *(unsigned*)(dst + f) = pack2(s0, s1);
    }
    return;
  }
  int row = blockIdx.x * 4 + (threadIdx.x >> 6), lane = threadIdx.x & 63;
  const u16* xr = x + (size_t)row * D;
  float v[9]; float s = 0.f;
  for (int i = 0; i < 9; ++i) { v[i] = bf2f(xr[lane + 64 * i]); s += v[i]; }
  for (int o = 32; o; o >>= 1) s += __shfl_xor(s, o);
  float m = s * (1.f / D);
  float q = 0.f;
  for (int i = 0; i < 9; ++i) { float d = v[i] - m; q += d * d; }
  for (int o = 32; o; o >>= 1) q += __shfl_xor(q, o);
  float r = rsqrtf(q * (1.f / D) + 1e-5f);
  for (int i = 0; i < 9; ++i) xn[(size_t)row * D + lane + 64 * i] = f2bf((v[i] - m) * r);
}

__device__ __forceinline__ void glds16_asm(const void* gsrc, unsigned lds_dst) {
  unsigned keep;
  asm volatile("s_mov_b32 %0, m0\n\ts_mov_b32 m0, %2\n\ts_nop 0\n\tglobal_load_lds_dwordx4 %1, off\n\ts_mov_b32 m0, %0" : "=&s"(keep) : "v"(gsrc), "s"(lds_dst) : "memory");
}
#define GLDS(g, l) glds16_asm((const void*)(g), (unsigned)(size_t)(l))
#define WAIT_V(n) asm volatile("s_waitcnt vmcnt(" #n ")" ::: "memory")
#define WAIT_L0() asm volatile("s_waitcnt lgkmcnt(0)" ::: "memory")
#define BAR() __builtin_amdgcn_s_barrier()

__device__ __forceinline__ int swap23(int r) { return (r & ~0xC) | ((r & 4) << 1) | ((r & 8) >> 1); }

template <int N> __device__ __forceinline__ void wait_v() { asm volatile("s_waitcnt vmcnt(%0)" :: "n"(N) : "memory"); }
template <int MT, int NTW, int NCH, int NS>
struct Core {
  static constexpr int PITCH = NCH * 16;
  static constexpr int NB = (32 * NCH + 63) / 64;
  static constexpr int STAGE = 8192 + NB * 1024;
  static constexpr int NLO = 1 + NB / 8, NHI = 1 + (NB + 7) / 8, XW = NB % 8;
  template <int K> static __device__ __forceinline__ void wait_stages(int wid) {
    if (XW == 0) wait_v<K * NLO>();
    else { if (wid < XW) wait_v<K * NHI>(); else wait_v<K * NLO>(); }
  }
};

template <int MT, int NTW, int NCH, int NS, int KID, class BSRC>
__device__ __forceinline__ void gemm_core(int NIT, char* smem, const u16* ag0, BSRC&& bsrc, f32x4 (&acc)[MT][NTW], int a_row0  , int tile0  ,
                                          int nsw, const u16* ag1) {
  using C = Core<MT, NTW, NCH, NS>;
  const int tid = threadIdx.x, lane = tid & 63;
  const int wid = __builtin_amdgcn_readfirstlane(tid >> 6);
  const int fr = lane & 15, fq = lane >> 4;
  const u16* ag = ag0;
  const u16* bg[4]; int bst[4];
#pragma unroll
  for (int i = 0; i < 4; ++i) {
    int ci = (wid + 8 * i) * 64 + lane;
    int rho = ci / NCH, c = ci - rho * NCH;
    if (rho > 31) { rho = 31; }
    bsrc(swap23(rho), c, bg[i], bst[i], 0);
  }
  int nissued = 0;
  auto issue = [&](int stg) {
    if (nissued == nsw) {
      ag = ag1;
#pragma unroll
      for (int i = 0; i < 4; ++i) {
        int ci = (wid + 8 * i) * 64 + lane;
        int rho = ci / NCH, c = ci - rho * NCH;
        if (rho > 31) { rho = 31; }
        bsrc(swap23(rho), c, bg[i], bst[i], 1);
      }
    }
    ++nissued;
    char* sb = smem + (stg % NS) * C::STAGE;
    GLDS(ag, sb + wid * 1024); ag += 32;
#pragma unroll
    for (int i = 0; i < 4; ++i) {
      if (8 * i < C::NB && (8 * i + 8 <= C::NB || wid < C::NB - 8 * i)) GLDS(bg[i], sb + 8192 + (wid + 8 * i) * 1024);
      bg[i] += bst[i];
    }
  };
  const int a_base = (a_row0 + fr) * 64 + ((fq ^ ((-(fr >> 2)) & 3)) * 16);
  const int b_base = 8192 + (16 * (fq >> 1) + 4 * (fq & 1) + (fr >> 2)) * C::PITCH + (2 * tile0 + ((lane & 3) >> 1)) * 16 + 8 * (lane & 1);
  bf16x8 afA[MT], bfA[NTW], afB[MT], bfB[NTW];
  auto ldfr = [&](int stg, bf16x8 (&af)[MT], bf16x8 (&bf)[NTW]) {
    typedef LDS_AS s16x4* lp;
    const char* nb = smem + (stg % NS) * C::STAGE;
#pragma unroll
    for (int j = 0; j < NTW; ++j) {
      s16x4 lo = __builtin_amdgcn_ds_read_tr16_b64_v4i16((lp)(nb + b_base + j * 32));
      s16x4 hi = __builtin_amdgcn_ds_read_tr16_b64_v4i16((lp)(nb + b_base + j * 32 + 8 * C::PITCH));
      bf[j] = __builtin_shufflevector(lo, hi, 0, 1, 2, 3, 4, 5, 6, 7);
    }
#pragma unroll
    for (int m = 0; m < MT; ++m) af[m] = *(const bf16x8*)(nb + a_base + m * 1024);
  };
  auto mma = [&](bf16x8 (&af)[MT], bf16x8 (&bf)[NTW]) {
#pragma unroll
    for (int j = 0; j < NTW; ++j)
#pragma unroll
      for (int m = 0; m < MT; ++m)
        acc[m][j] = __builtin_amdgcn_mfma_f32_16x16x32_bf16(bf[j], af[m], acc[m][j], 0, 0, 0);
  };
  constexpr int NRD = 2 * NTW + MT, NMM = MT * NTW;
  constexpr int MPR = NMM / NRD > 0 ? NMM / NRD : 1;
#define CORE_IL() do { _Pragma("unroll") for (int _i = 0; _i < NRD; ++_i) { __builtin_amdgcn_sched_group_barrier(0x100, 1, 0); __builtin_amdgcn_sched_group_barrier(0x8, MPR, 0); } __builtin_amdgcn_sched_group_barrier(0x8, 64, 0); } while (0)
  STAMPK(KID, 0);
#pragma unroll
  for (int s = 0; s < NS - 1; ++s) issue(s);
  C::template wait_stages<NS - 2>(wid);
  BAR();
  STAMPK(KID, 1);
  ldfr(0, afA, bfA);
#pragma unroll 1
  for (int t = 0; t + NS < NIT; t += 2) {
    C::template wait_stages<NS - 3>(wid); BAR();
    issue(t + NS - 1);
    ldfr(t + 1, afB, bfB); mma(afA, bfA); CORE_IL();
    C::template wait_stages<NS - 3>(wid); BAR();
    issue(t + NS);
    ldfr(t + 2, afA, bfA); mma(afB, bfB); CORE_IL();
  }
  static_assert(NS == 4 || NS == 8, "NS");
  C::template wait_stages<NS - 3>(wid); BAR();
  issue(NIT - 1);
  ldfr(NIT - NS + 1, afB, bfB); mma(afA, bfA); CORE_IL();
  if (NS == 8) {
    C::template wait_stages<5>(wid); BAR(); ldfr(NIT - 6, afA, bfA); mma(afB, bfB); CORE_IL();
    C::template wait_stages<4>(wid); BAR(); ldfr(NIT - 5, afB, bfB); mma(afA, bfA); CORE_IL();
    C::template wait_stages<3>(wid); BAR(); ldfr(NIT - 4, afA, bfA); mma(afB, bfB); CORE_IL();
    C::template wait_stages<2>(wid); BAR(); ldfr(NIT - 3, afB, bfB); mma(afA, bfA); CORE_IL();
  }
  C::template wait_stages<1>(wid); BAR(); ldfr(NIT - 2, afA, bfA); mma(afB, bfB); CORE_IL();
  C::template wait_stages<0>(wid); BAR(); ldfr(NIT - 1, afB, bfB); mma(afA, bfA); CORE_IL();
  mma(afB, bfB);
  STAMPK(KID, 2);
}

#define SCHED_IL() do { _Pragma("unroll") for (int _i = 0; _i < 9; ++_i) { __builtin_amdgcn_sched_group_barrier(0x100, 2, 0); __builtin_amdgcn_sched_group_barrier(0x8, 1, 0); } __builtin_amdgcn_sched_group_barrier(0x8, 32, 0); } while (0)

__device__ __forceinline__ bf16x8 tr_frag(const char* p) {
  typedef LDS_AS s16x4* lp;
  s16x4 lo = __builtin_amdgcn_ds_read_tr16_b64_v4i16((lp)(p));
  s16x4 hi = __builtin_amdgcn_ds_read_tr16_b64_v4i16((lp)(p + 4096));
  return __builtin_shufflevector(lo, hi, 0, 1, 2, 3, 4, 5, 6, 7);
}

template <int TYPE>
__device__ __forceinline__ void l1_wg(char* smem, const u16* __restrict__ xn, const u16* __restrict__ w1, const u16* __restrict__ wqkv,
                                      const u16* __restrict__ wf, u16* __restrict__ hbuf, u16* __restrict__ qb, float* __restrict__ kvslab,
                                      int rt, int ct) {
  constexpr int NCH = TYPE ? 38 : 54;
  constexpr int NTW = TYPE ? 5 : 7;
  const int tid = threadIdx.x, lane = tid & 63;
  const int wid = __builtin_amdgcn_readfirstlane(tid >> 6);
  const int wr = wid >> 2, wc = wid & 3, fr = lane & 15, fq = lane >> 4;
  const u16* ag = xn + (size_t)(rt * 128 + wid * 16 + (lane >> 2)) * D + (((lane & 3) ^ ((-(lane >> 4)) & 3)) * 8);
  f32x4 acc[4][NTW];
#pragma unroll
  for (int m = 0; m < 4; ++m)
#pragma unroll
    for (int j = 0; j < NTW; ++j) acc[m][j] = f32x4{0.f, 0.f, 0.f, 0.f};
  auto bsrc = [&](int k, int c, const u16*& p, int& st, int phase) {
    if (TYPE == 0) {
      int gc = ct * 432 + c * 8;
      if (gc < F) { p = w1 + (size_t)k * F + gc; st = 32 * F; }
      else { p = wf + (size_t)k * (2 * D) + (gc - F); st = 32 * 2 * D; }
    } else {
      if (c > 35) c = 35;
      int part = c >= 18 ? 1 : 0, cc = c - 18 * part;
      if (part == 0) { p = wf + (size_t)k * (2 * D) + D + ct * 144 + cc * 8; st = 32 * 2 * D; }
      else { p = wqkv + (size_t)k * (3 * D) + 2 * D + ct * 144 + cc * 8; st = 32 * 3 * D; }
    }
  };
  gemm_core<4, NTW, NCH, 4, 1>(18, smem, ag, bsrc, acc, wr * 64, NTW * wc, 1 << 30, nullptr);
  if (TYPE == 0) {
#pragma unroll
    for (int m = 0; m < 4; ++m)
#pragma unroll
      for (int j = 0; j < NTW; ++j) {
        int T = NTW * wc + j;
        if (T < 27) {
          int row = rt * 128 + wr * 64 + m * 16 + fr;
          int col0 = ct * 432 + T * 16 + fq * 4;
          f32x4 v = acc[m][j];
          if (col0 < F) {
            float o[4];
#pragma unroll
            for (int e = 0; e < 4; ++e) o[e] = v[e] * __builtin_amdgcn_rcpf(1.f + __builtin_amdgcn_exp2f(v[e] * -1.44269504f));
            uint2 pk = make_uint2(pack2(o[0], o[1]), pack2(o[2], o[3]));
            *(uint2*)(hbuf + (size_t)row * F + col0) = pk;
          } else {
            uint2 pk = make_uint2(pack2(v[0], v[1]), pack2(v[2], v[3]));
            *(uint2*)(qb + (size_t)row * D + (col0 - F)) = pk;
          }
        }
      }
  } else {
    constexpr int PK_PITCH = 608, VT_PITCH = 352;
    char* Pk = smem;
    char* Vt = smem + 128 * PK_PITCH;
    __syncthreads();
#pragma unroll
    for (int m = 0; m < 4; ++m)
#pragma unroll
      for (int j = 0; j < NTW; ++j) {
        int T = NTW * wc + j;
        if (T < 18) {
          int row = wr * 64 + m * 16 + fr;
#pragma unroll
          for (int e = 0; e < 4; ++e) {
            int c = T * 16 + fq * 4 + e;
            int part = c >= 144 ? 1 : 0, cc = c - 144 * part, hd = cc / 18, d = cc - hd * 18;
            u16 val = f2bf(acc[m][j][e]);
            if (part == 0) *(u16*)(Pk + row * PK_PITCH + (hd * 36 + d) * 2) = val;
            else *(u16*)(Vt + row * VT_PITCH + (hd * 20 + d) * 2) = val;
          }
        }
      }
    for (int i = tid; i < 128 * 8; i += 512) { int row = i >> 3, hd = i & 7; *(u16*)(Vt + row * VT_PITCH + (hd * 20 + 18) * 2) = 0x3f80; }
    __syncthreads();
    {
      {
        unsigned* pr0 = (unsigned*)(Pk + lane * PK_PITCH + wid * 72);
        unsigned* pr1 = (unsigned*)(Pk + (lane + 64) * PK_PITCH + wid * 72);
        float p0[18], p1[18];
#pragma unroll
        for (int i = 0; i < 9; ++i) {
          unsigned u = pr0[i]; p0[2 * i] = __uint_as_float(u << 16); p0[2 * i + 1] = __uint_as_float(u & 0xffff0000u);
          unsigned w = pr1[i]; p1[2 * i] = __uint_as_float(w << 16); p1[2 * i + 1] = __uint_as_float(w & 0xffff0000u);
        }
        auto finish = [&](float* p, unsigned* pr) {
          float mx = -1e30f, mn = 1e30f;
#pragma unroll
          for (int f = 0; f < 18; ++f) { mx = fmaxf(mx, p[f]); mn = fminf(mn, p[f]); }
          float e1[18], e2[18], s1 = 0.f, s2 = 0.f;
#pragma unroll
          for (int f = 0; f < 18; ++f) { e1[f] = __expf(p[f] - mx); s1 += e1[f]; e2[f] = __expf(mn - p[f]); s2 += e2[f]; }
          float r1 = 1.f / s1, r2 = 1.f / s2;
#pragma unroll
          for (int i = 0; i < 9; ++i) { pr[i] = pack2(e1[2 * i] * r1, e1[2 * i + 1] * r1); pr[9 + i] = pack2(e2[2 * i] * r2, e2[2 * i + 1] * r2); }
        };
        finish(p0, pr0);
        finish(p1, pr1);
      }
    }
    __syncthreads();
    {
      typedef LDS_AS s16x4* lp;
      f32x4 kvacc[3][2];
#pragma unroll
      for (int a = 0; a < 3; ++a) for (int b = 0; b < 2; ++b) kvacc[a][b] = f32x4{0.f, 0.f, 0.f, 0.f};
      const int q = fr >> 2, p = lane & 3;
#pragma unroll
      for (int ks = 0; ks < 4; ++ks) {
        bf16x8 pa[3], vb[2];
        int r0 = ks * 32 + 8 * fq + q;
#pragma unroll
        for (int mt = 0; mt < 3; ++mt) {
          const char* a0 = Pk + r0 * PK_PITCH + (wid * 36 + mt * 16 + 4 * p) * 2;
          s16x4 lo = __builtin_amdgcn_ds_read_tr16_b64_v4i16((lp)(a0));
          s16x4 hi = __builtin_amdgcn_ds_read_tr16_b64_v4i16((lp)(a0 + 4 * PK_PITCH));
          pa[mt] = __builtin_shufflevector(lo, hi, 0, 1, 2, 3, 4, 5, 6, 7);
        }
#pragma unroll
        for (int nt = 0; nt < 2; ++nt) {
          const char* b0 = Vt + r0 * VT_PITCH + (wid * 20 + nt * 16 + 4 * p) * 2;
          s16x4 lo = __builtin_amdgcn_ds_read_tr16_b64_v4i16((lp)(b0));
          s16x4 hi = __builtin_amdgcn_ds_read_tr16_b64_v4i16((lp)(b0 + 4 * VT_PITCH));
          vb[nt] = __builtin_shufflevector(lo, hi, 0, 1, 2, 3, 4, 5, 6, 7);
        }
#pragma unroll
        for (int mt = 0; mt < 3; ++mt)
#pragma unroll
          for (int nt = 0; nt < 2; ++nt) kvacc[mt][nt] = __builtin_amdgcn_mfma_f32_16x16x32_bf16(pa[mt], vb[nt], kvacc[mt][nt], 0, 0, 0);
      }
      float* dst = kvslab + ((size_t)rt * 32 + ct * 8 + wid) * 720;
#pragma unroll
      for (int mt = 0; mt < 3; ++mt)
#pragma unroll
        for (int nt = 0; nt < 2; ++nt)
#pragma unroll
          for (int e = 0; e < 4; ++e) {
            int f = mt * 16 + fq * 4 + e, d = nt * 16 + fr;
            if (f < 36 && d < 19) dst[f * 20 + d] = kvacc[mt][nt][e];
          }
    }
  }
}

__global__ __launch_bounds__(512, 2) void k_l1(const u16* __restrict__ xn, const u16* __restrict__ w1, const u16* __restrict__ wqkv,
                                               const u16* __restrict__ wf, u16* __restrict__ hbuf, u16* __restrict__ qb, float* __restrict__ kvslab) {
  __shared__ __attribute__((aligned(16))) char smem[163840];
  int bid = blockIdx.x, xcd = bid & 7, idx = bid >> 3;
  int rt = (xcd & 1) * 8 + (idx & 7);
  int cs = (xcd >> 1) * 4 + (idx >> 3);
  if (cs < 12) l1_wg<0>(smem, xn, w1, wqkv, wf, hbuf, qb, kvslab, rt, cs);
  else l1_wg<1>(smem, xn, w1, wqkv, wf, hbuf, qb, kvslab, rt, cs - 12);
  STAMPK(1, 3);
}

constexpr int G4_A = 16384, G4_B = 18432, G4_STAGE = G4_A + G4_B;
__global__ __launch_bounds__(512, 2) void k_g2(const u16* __restrict__ hbuf, const u16* __restrict__ w2, const u16* __restrict__ o, const u16* __restrict__ wo,
                                               float* __restrict__ slab) {
  __shared__ __attribute__((aligned(16))) char smem[4 * G4_STAGE];
  int bid = blockIdx.x, xcd = bid & 7, idx = bid >> 3;
  int ks = xcd >> 1, rt = (xcd & 1) * 8 + (idx & 7), ct = idx >> 3;
  const int tid = threadIdx.x, lane = tid & 63, fr = lane & 15, fq = lane >> 4;
  const int wid = __builtin_amdgcn_readfirstlane(tid >> 6), wm = wid & 1, g = (wid >> 1) & 1, wn = wid >> 2;
  f32x4 acc[4][5];
#pragma unroll
  for (int m = 0; m < 4; ++m)
#pragma unroll
    for (int j = 0; j < 5; ++j) acc[m][j] = f32x4{0.f, 0.f, 0.f, 0.f};
  const int nst = ks == 3 ? 21 : 20, nsw = ks == 3 ? 12 : 100;
  const u16* ap[2]; const u16* bp[3];
  STAMPK(2, 0);
  auto setp = [&](const u16* ab, int lda, const u16* bb) {
#pragma unroll
    for (int i = 0; i < 2; ++i) {
      int row = (wid * 2 + i) * 8 + (lane >> 3), lc = (lane & 7) ^ ((row >> 1) & 7);
      ap[i] = ab + (size_t)(rt * 128 + row) * lda + lc * 8;
    }
#pragma unroll
    for (int i = 0; i < 3; ++i) {
      int ci = (wid + 8 * i) * 64 + lane; if (ci > 1151) ci = 1151;
      int rp = ci / 18, c = ci - rp * 18, k = (rp & ~31) | swap23(rp & 31);
      bp[i] = bb + (size_t)k * D + ct * 144 + c * 8;
    }
  };
  setp(hbuf + ks * 1280, F, w2 + (size_t)ks * 1280 * D);
  auto issue = [&](int t) {
    if (t == nsw) setp(o, D, wo);
    char* sb = smem + (t & 3) * G4_STAGE;
#pragma unroll
    for (int i = 0; i < 2; ++i) { GLDS(ap[i], sb + (wid * 2 + i) * 1024); ap[i] += 64; }
#pragma unroll
    for (int i = 0; i < 3; ++i) { if (i < 2 || wid < 2) GLDS(bp[i], sb + G4_A + (wid + 8 * i) * 1024); bp[i] += (size_t)64 * D; }
  };
  const int a_base = (wm * 64 + fr) * 128 + (((g * 4 + fq) ^ ((fr >> 1) & 7)) * 16);
  const int b_base = G4_A + (g * 32 + 16 * (fq >> 1) + 4 * (fq & 1) + (fr >> 2)) * 288 + (10 * wn + ((lane & 3) >> 1)) * 16 + 8 * (lane & 1);
  bf16x8 afA[4], bfA[5], afB[4], bfB[5];
  auto ldfr = [&](int t, bf16x8 (&af)[4], bf16x8 (&bf)[5]) {
    typedef LDS_AS s16x4* lp;
    const char* nb = smem + (t & 3) * G4_STAGE;
#pragma unroll
    for (int j = 0; j < 5; ++j) {
      s16x4 lo = __builtin_amdgcn_ds_read_tr16_b64_v4i16((lp)(nb + b_base + j * 32));
      s16x4 hi = __builtin_amdgcn_ds_read_tr16_b64_v4i16((lp)(nb + b_base + j * 32 + 8 * 288));
      bf[j] = __builtin_shufflevector(lo, hi, 0, 1, 2, 3, 4, 5, 6, 7);
    }
#pragma unroll
    for (int m = 0; m < 4; ++m) af[m] = *(const bf16x8*)(nb + a_base + m * 2048);
  };
  auto mma = [&](bf16x8 (&af)[4], bf16x8 (&bf)[5]) {
#pragma unroll
    for (int j = 0; j < 5; ++j)
#pragma unroll
      for (int m = 0; m < 4; ++m) acc[m][j] = __builtin_amdgcn_mfma_f32_16x16x32_bf16(bf[j], af[m], acc[m][j], 0, 0, 0);
  };
  auto waitst = [&](bool one) {
    if (wid < 2) { if (one) wait_v<5>(); else wait_v<0>(); } else { if (one) wait_v<4>(); else wait_v<0>(); }
  };
#define G4_IL() do { _Pragma("unroll") for (int _i = 0; _i < 14; ++_i) { __builtin_amdgcn_sched_group_barrier(0x100, 1, 0); __builtin_amdgcn_sched_group_barrier(0x8, 1, 0); } __builtin_amdgcn_sched_group_barrier(0x8, 64, 0); } while (0)
  issue(0); issue(1); issue(2);
  if (wid < 2) wait_v<10>(); else wait_v<8>();
  BAR();
  STAMPK(2, 1);
  ldfr(0, afA, bfA);
  int t = 0;
#pragma unroll 1
  for (; t + 2 < nst; t += 2) {
    waitst(t + 3 <= nst); BAR();
    if (t + 3 < nst) issue(t + 3);
    ldfr(t + 1, afB, bfB); mma(afA, bfA); G4_IL();
    waitst(t + 4 <= nst); BAR();
    if (t + 4 < nst) issue(t + 4);
    ldfr(t + 2, afA, bfA); mma(afB, bfB); G4_IL();
  }
  if (t + 1 < nst) {
    wait_v<0>(); BAR();
    ldfr(t + 1, afB, bfB); mma(afA, bfA); G4_IL();
    mma(afB, bfB);
  } else mma(afA, bfA);
  STAMPK(2, 2);
  __syncthreads();
  f32x4* red = (f32x4*)smem;
  const int rs = (wn * 2 + wm) * 20;
  if (g == 1) {
#pragma unroll
    for (int m = 0; m < 4; ++m)
#pragma unroll
      for (int j = 0; j < 5; ++j) red[(rs + m * 5 + j) * 64 + lane] = acc[m][j];
  }
  __syncthreads();
  if (g == 0) {
    float* dst = slab + (size_t)ks * S * D;
#pragma unroll
    for (int m = 0; m < 4; ++m)
#pragma unroll
      for (int j = 0; j < 5; ++j)
        if (wn == 0 || j < 4) *(f32x4*)(dst + (size_t)(rt * 128 + wm * 64 + m * 16 + fr) * D + ct * 144 + (wn * 5 + j) * 16 + fq * 4) = acc[m][j] + red[(rs + m * 5 + j) * 64 + lane];
  }
  STAMPK(2, 3);
}

__global__ __launch_bounds__(256) void k_l3(const float* __restrict__ slab, const u16* __restrict__ x, u16* __restrict__ out) {
  int i = (blockIdx.x * 256 + threadIdx.x) * 4;
  f32x4 v = *(const f32x4*)(slab + i);
#pragma unroll
  for (int k = 1; k < 4; ++k) v += *(const f32x4*)(slab + (size_t)k * S * D + i);
  uint2 xv = *(const uint2*)(x + i);
  float x0 = __uint_as_float(xv.x << 16), x1 = __uint_as_float(xv.x & 0xffff0000u), x2 = __uint_as_float(xv.y << 16), x3 = __uint_as_float(xv.y & 0xffff0000u);
  *(uint2*)(out + i) = make_uint2(pack2(v[0] + x0, v[1] + x1), pack2(v[2] + x2, v[3] + x3));
}

__global__ __launch_bounds__(256) void k_qo3(const u16* __restrict__ pq, const float* __restrict__ kvslab, u16* __restrict__ o) {
  __shared__ __attribute__((aligned(16))) u16 kvT[32 * 72];
  const int h = blockIdx.x >> 3, rg = blockIdx.x & 7, tid = threadIdx.x, lane = tid & 63, wid = tid >> 6;
  const int fr = lane & 15, fq = lane >> 4;
  const int base = fq * 5 - (fq == 3 ? 1 : 0), cnt = fq < 2 ? 5 : 4;
  float p[4][5];
#pragma unroll
  for (int mt = 0; mt < 4; ++mt) {
    const u16* pr = pq + (size_t)(rg * 256 + wid * 64 + mt * 16 + fr) * D + h * DH + base;
#pragma unroll
    for (int j = 0; j < 5; ++j) p[mt][j] = bf2f(pr[j < cnt ? j : 0]);
  }
  {
    float v[3][16];
#pragma unroll
    for (int i = 0; i < 3; ++i) {
      int e = tid + 256 * i; if (e > 683) e = 683;
      int f = e / 19, n = e - f * 19;
#pragma unroll
      for (int r = 0; r < 16; ++r) v[i][r] = kvslab[((size_t)r * 32 + h) * 720 + f * 20 + n];
    }
    for (int i = tid; i < 32 * 36; i += 256) ((unsigned*)kvT)[i] = 0u;
    __syncthreads();
#pragma unroll
    for (int i = 0; i < 3; ++i) {
      int e = tid + 256 * i;
      float t = 0.f;
#pragma unroll
      for (int r = 0; r < 16; ++r) t += v[i][r];
      if (e < 684) {
        int f = e / 19, n = e - f * 19;
        int s = f >= 18 ? 1 : 0, d = f - 18 * s;
        int q = d < 10 ? d / 5 : 2 + (d - 10) / 4;
        int j = d - (q * 5 - (q == 3 ? 1 : 0));
        kvT[n * 72 + s * 32 + q * 8 + j] = f2bf(t);
      }
    }
  }
  __syncthreads();
  bf16x8 bfr[2][2];
#pragma unroll
  for (int s = 0; s < 2; ++s)
#pragma unroll
    for (int j = 0; j < 2; ++j) bfr[s][j] = *(const bf16x8*)(kvT + (j * 16 + fr) * 72 + s * 32 + fq * 8);
#pragma unroll
  for (int mt = 0; mt < 4; ++mt) {
    float mx = p[mt][0], mn = p[mt][0];
#pragma unroll
    for (int j = 1; j < 5; ++j) { mx = fmaxf(mx, p[mt][j]); mn = fminf(mn, p[mt][j]); }
    mx = fmaxf(mx, __shfl_xor(mx, 16)); mx = fmaxf(mx, __shfl_xor(mx, 32));
    mn = fminf(mn, __shfl_xor(mn, 16)); mn = fminf(mn, __shfl_xor(mn, 32));
    float e1[5], e2[5], s1 = 0.f, s2 = 0.f;
#pragma unroll
    for (int j = 0; j < 5; ++j) {
      e1[j] = j < cnt ? __expf(p[mt][j] - mx) : 0.f; e2[j] = j < cnt ? __expf(mn - p[mt][j]) : 0.f;
      s1 += e1[j]; s2 += e2[j];
    }
    s1 += __shfl_xor(s1, 16); s1 += __shfl_xor(s1, 32);
    s2 += __shfl_xor(s2, 16); s2 += __shfl_xor(s2, 32);
    float r1 = 1.f / s1, r2 = 1.f / s2;
    typedef __attribute__((ext_vector_type(4))) unsigned u32x4;
    u32x4 a0 = {pack2(e1[0] * r1, e1[1] * r1), pack2(e1[2] * r1, e1[3] * r1), pack2(e1[4] * r1, 0.f), 0u};
    u32x4 a1 = {pack2(e2[0] * r2, e2[1] * r2), pack2(e2[2] * r2, e2[3] * r2), pack2(e2[4] * r2, 0.f), 0u};
    bf16x8 af0 = __builtin_bit_cast(bf16x8, a0), af1 = __builtin_bit_cast(bf16x8, a1);
    f32x4 acc[2];
#pragma unroll
    for (int j = 0; j < 2; ++j) {
      acc[j] = f32x4{0.f, 0.f, 0.f, 0.f};
      acc[j] = __builtin_amdgcn_mfma_f32_16x16x32_bf16(bfr[0][j], af0, acc[j], 0, 0, 0);
      acc[j] = __builtin_amdgcn_mfma_f32_16x16x32_bf16(bfr[1][j], af1, acc[j], 0, 0, 0);
    }
    float den = rbf(__shfl(acc[1][2], fr)) + 1e-6f;
    float rd = 1.f / den;
    unsigned* dst = (unsigned*)(o + (size_t)(rg * 256 + wid * 64 + mt * 16 + fr) * D + h * DH);
    dst[2 * fq] = pack2(rbf(acc[0][0]) * rd, rbf(acc[0][1]) * rd);
    dst[2 * fq + 1] = pack2(rbf(acc[0][2]) * rd, rbf(acc[0][3]) * rd);
    if (fq == 0) dst[8] = pack2(rbf(acc[1][0]) * rd, rbf(acc[1][1]) * rd);
  }
}

extern "C" void kernel_launch(void* const* d_in, const int* in_sizes, int n_in, void* d_out, int out_size, void* d_ws, size_t ws_size, hipStream_t stream) {
  const u16* x = (const u16*)d_in[0]; const u16* wqkv = (const u16*)d_in[1]; const u16* wphi = (const u16*)d_in[2]; const u16* wo = (const u16*)d_in[3];
  const u16* w1 = (const u16*)d_in[5]; const u16* w2 = (const u16*)d_in[6];
  u16* out = (u16*)d_out;
  char* ws = (char*)d_ws;
  u16* xn = (u16*)ws; ws += (size_t)S * D * 2;
  u16* qb = (u16*)ws; ws += (size_t)S * D * 2;
  u16* hbuf = (u16*)ws; ws += (size_t)S * F * 2;
  float* kvslab = (float*)ws; ws += 16 * 32 * 720 * 4;
  float* kv = (float*)ws; ws += 32 * 720 * 4;
  u16* o = (u16*)ws; ws += (size_t)S * D * 2;
  float* slab = (float*)ws; ws += (size_t)4 * S * D * 4;
  u16* wf = (u16*)ws; ws += (size_t)D * 2 * D * 2;
  k_ln<<<S / 4 + 144, 256, 0, stream>>>(x, xn, wqkv, wphi, wf);
  k_l1<<<256, 512, 0, stream>>>(xn, w1, wqkv, wf, hbuf, qb, kvslab);
  k_qo3<<<256, 256, 0, stream>>>(qb, kvslab, o);
  k_g2<<<256, 512, 0, stream>>>(hbuf, w2, o, wo, slab);
  k_l3<<<S * D / 1024, 256, 0, stream>>>(slab, x, out);
}
```

```cpp
#include <hip/hip_runtime.h>
#include <hip/hip_bf16.h>
#include <stdint.h>

typedef __attribute__((ext_vector_type(8))) short bf16x8;
typedef __attribute__((ext_vector_type(4))) short s16x4;
typedef __attribute__((ext_vector_type(4))) float f32x4;
typedef unsigned short u16;
#define LDS_AS __attribute__((address_space(3)))

__device__ __forceinline__ float bf2f(u16 v) { return __uint_as_float(((unsigned)v) << 16); }
__device__ __forceinline__ u16 f2bf(float f) {
  unsigned u = __float_as_uint(f);
  u += 0x7fffu + ((u >> 16) & 1u);
  return (u16)(u >> 16);
}
__device__ __forceinline__ unsigned pack2(float lo, float hi) { unsigned r; asm("v_cvt_pk_bf16_f32 %0, %1, %2" : "=v"(r) : "v"(lo), "v"(hi)); return r; }
__device__ __forceinline__ float rbf(float f) { return __uint_as_float(pack2(f, f) << 16); }

constexpr int S = 2048, D = 576, H = 32, DH = 18, F = 4608;

#define STAMPK(k, i)
__global__ __launch_bounds__(256) void k_ln(const u16* __restrict__ x, u16* __restrict__ xn, const u16* __restrict__ wqkv, const u16* __restrict__ wphi, u16* __restrict__ wf,
                                            const u16* __restrict__ w1, const u16* __restrict__ w2) {
  if (blockIdx.x >= S / 4 + 144) {
    int t = (blockIdx.x - S / 4 - 144) * 256 + threadIdx.x;
    const uint4* p1 = (const uint4*)w1; const uint4* p2 = (const uint4*)w2;
    unsigned a = 0;
#pragma unroll
    for (int i = 0; i < 10; ++i) { uint4 v = p1[t + i * 32768 < 331776 ? t + i * 32768 : 0]; uint4 u = p2[t + i * 32768 < 331776 ? t + i * 32768 : 0]; a ^= v.x ^ v.w ^ u.x ^ u.w; }
    if (a == 0x9e3779b9u) wf[0] = 0;
    return;
  }
  if (blockIdx.x >= S / 4) {
    int u = __builtin_amdgcn_readfirstlane((blockIdx.x - S / 4) * 4 + (threadIdx.x >> 6));
    int wh = u & 63, k = (u >> 6) * 64 + (threadIdx.x & 63), h = wh & 31;
    const u16* src = wqkv + (size_t)k * (3 * D) + wh * 18;
    const unsigned* W = (const unsigned*)(wphi + h * 324);
    float a[18];
#pragma unroll
    for (int i = 0; i < 9; ++i) { unsigned v = *(const unsigned*)(src + 2 * i); a[2 * i] = __uint_as_float(v << 16); a[2 * i + 1] = __uint_as_float(v & 0xffff0000u); }
    u16* dst = wf + (size_t)k * (2 * D) + wh * 18;
#pragma unroll
    for (int f = 0; f < 18; f += 2) {
      float s0 = 0.f, s1 = 0.f;
#pragma unroll
      for (int d = 0; d < 18; ++d) { unsigned w = W[d * 9 + (f >> 1)]; s0 = fmaf(a[d], __uint_as_float(w << 16), s0); s1 = fmaf(a[d], __uint_as_float(w & 0xffff0000u), s1); }
      *(unsigned*)(dst + f) = pack2(s0, s1);
    }
    return;
  }
  int row = blockIdx.x * 4 + (threadIdx.x >> 6), lane = threadIdx.x & 63;
  const u16* xr = x + (size_t)row * D;
  float v[9]; float s = 0.f;
  for (int i = 0; i < 9; ++i) { v[i] = bf2f(xr[lane + 64 * i]); s += v[i]; }
  for (int o = 32; o; o >>= 1) s += __shfl_xor(s, o);
  float m = s * (1.f / D);
  float q = 0.f;
  for (int i = 0; i < 9; ++i) { float d = v[i] - m; q += d * d; }
  for (int o = 32; o; o >>= 1) q += __shfl_xor(q, o);
  float r = rsqrtf(q * (1.f / D) + 1e-5f);
  for (int i = 0; i < 9; ++i) xn[(size_t)row * D + lane + 64 * i] = f2bf((v[i] - m) * r);
}

__device__ __forceinline__ void glds16_asm(const void* gsrc, unsigned lds_dst) {
  unsigned keep;
  asm volatile("s_mov_b32 %0, m0\n\ts_mov_b32 m0, %2\n\ts_nop 0\n\tglobal_load_lds_dwordx4 %1, off\n\ts_mov_b32 m0, %0" : "=&s"(keep) : "v"(gsrc), "s"(lds_dst) : "memory");
}
#define GLDS(g, l) glds16_asm((const void*)(g), (unsigned)(size_t)(l))
#define WAIT_V(n) asm volatile("s_waitcnt vmcnt(" #n ")" ::: "memory")
#define WAIT_L0() asm volatile("s_waitcnt lgkmcnt(0)" ::: "memory")
#define BAR() __builtin_amdgcn_s_barrier()

__device__ __forceinline__ int swap23(int r) { return (r & ~0xC) | ((r & 4) << 1) | ((r & 8) >> 1); }

template <int N> __device__ __forceinline__ void wait_v() { asm volatile("s_waitcnt vmcnt(%0)" :: "n"(N) : "memory"); }
template <int MT, int NTW, int NCH, int NS>
struct Core {
  static constexpr int PITCH = NCH * 16;
  static constexpr int NB = (32 * NCH + 63) / 64;
  static constexpr int STAGE = 8192 + NB * 1024;
  static constexpr int NLO = 1 + NB / 8, NHI = 1 + (NB + 7) / 8, XW = NB % 8;
  template <int K> static __device__ __forceinline__ void wait_stages(int wid) {
    if (XW == 0) wait_v<K * NLO>();
    else { if (wid < XW) wait_v<K * NHI>(); else wait_v<K * NLO>(); }
  }
};

template <int MT, int NTW, int NCH, int NS, int KID, class BSRC>
__device__ __forceinline__ void gemm_core(int NIT, char* smem, const u16* ag0, BSRC&& bsrc, f32x4 (&acc)[MT][NTW], int a_row0  , int tile0  ,
                                          int nsw, const u16* ag1) {
  using C = Core<MT, NTW, NCH, NS>;
  const int tid = threadIdx.x, lane = tid & 63;
  const int wid = __builtin_amdgcn_readfirstlane(tid >> 6);
  const int fr = lane & 15, fq = lane >> 4;
  const u16* ag = ag0;
  const u16* bg[4]; int bst[4];
#pragma unroll
  for (int i = 0; i < 4; ++i) {
    int ci = (wid + 8 * i) * 64 + lane;
    int rho = ci / NCH, c = ci - rho * NCH;
    if (rho > 31) { rho = 31; }
    bsrc(swap23(rho), c, bg[i], bst[i], 0);
  }
  int nissued = 0;
  auto issue = [&](int stg) {
    if (nissued == nsw) {
      ag = ag1;
#pragma unroll
      for (int i = 0; i < 4; ++i) {
        int ci = (wid + 8 * i) * 64 + lane;
        int rho = ci / NCH, c = ci - rho * NCH;
        if (rho > 31) { rho = 31; }
        bsrc(swap23(rho), c, bg[i], bst[i], 1);
      }
    }
    ++nissued;
    char* sb = smem + (stg % NS) * C::STAGE;
    GLDS(ag, sb + wid * 1024); ag += 32;
#pragma unroll
    for (int i = 0; i < 4; ++i) {
      if (8 * i < C::NB && (8 * i + 8 <= C::NB || wid < C::NB - 8 * i)) GLDS(bg[i], sb + 8192 + (wid + 8 * i) * 1024);
      bg[i] += bst[i];
    }
  };
  const int a_base = (a_row0 + fr) * 64 + ((fq ^ ((-(fr >> 2)) & 3)) * 16);
  const int b_base = 8192 + (16 * (fq >> 1) + 4 * (fq & 1) + (fr >> 2)) * C::PITCH + (2 * tile0 + ((lane & 3) >> 1)) * 16 + 8 * (lane & 1);
  bf16x8 afA[MT], bfA[NTW], afB[MT], bfB[NTW];
  auto ldfr = [&](int stg, bf16x8 (&af)[MT], bf16x8 (&bf)[NTW]) {
    typedef LDS_AS s16x4* lp;
    const char* nb = smem + (stg % NS) * C::STAGE;
#pragma unroll
    for (int j = 0; j < NTW; ++j) {
      s16x4 lo = __builtin_amdgcn_ds_read_tr16_b64_v4i16((lp)(nb + b_base + j * 32));
      s16x4 hi = __builtin_amdgcn_ds_read_tr16_b64_v4i16((lp)(nb + b_base + j * 32 + 8 * C::PITCH));
      bf[j] = __builtin_shufflevector(lo, hi, 0, 1, 2, 3, 4, 5, 6, 7);
    }
#pragma unroll
    for (int m = 0; m < MT; ++m) af[m] = *(const bf16x8*)(nb + a_base + m * 1024);
  };
  auto mma = [&](bf16x8 (&af)[MT], bf16x8 (&bf)[NTW]) {
#pragma unroll
    for (int j = 0; j < NTW; ++j)
#pragma unroll
      for (int m = 0; m < MT; ++m)
        acc[m][j] = __builtin_amdgcn_mfma_f32_16x16x32_bf16(bf[j], af[m], acc[m][j], 0, 0, 0);
  };
  constexpr int NRD = 2 * NTW + MT, NMM = MT * NTW;
  constexpr int MPR = NMM / NRD > 0 ? NMM / NRD : 1;
#define CORE_IL() do { _Pragma("unroll") for (int _i = 0; _i < NRD; ++_i) { __builtin_amdgcn_sched_group_barrier(0x100, 1, 0); __builtin_amdgcn_sched_group_barrier(0x8, MPR, 0); } __builtin_amdgcn_sched_group_barrier(0x8, 64, 0); } while (0)
  STAMPK(KID, 0);
#pragma unroll
  for (int s = 0; s < NS - 1; ++s) issue(s);
  C::template wait_stages<NS - 2>(wid);
  BAR();
  STAMPK(KID, 1);
  ldfr(0, afA, bfA);
#pragma unroll 1
  for (int t = 0; t + NS < NIT; t += 2) {
    C::template wait_stages<NS - 3>(wid); BAR();
    issue(t + NS - 1);
    ldfr(t + 1, afB, bfB); mma(afA, bfA); CORE_IL();
    C::template wait_stages<NS - 3>(wid); BAR();
    issue(t + NS);
    ldfr(t + 2, afA, bfA); mma(afB, bfB); CORE_IL();
  }
  static_assert(NS == 4 || NS == 8, "NS");
  C::template wait_stages<NS - 3>(wid); BAR();
  issue(NIT - 1);
  ldfr(NIT - NS + 1, afB, bfB); mma(afA, bfA); CORE_IL();
  if (NS == 8) {
    C::template wait_stages<5>(wid); BAR(); ldfr(NIT - 6, afA, bfA); mma(afB, bfB); CORE_IL();
    C::template wait_stages<4>(wid); BAR(); ldfr(NIT - 5, afB, bfB); mma(afA, bfA); CORE_IL();
    C::template wait_stages<3>(wid); BAR(); ldfr(NIT - 4, afA, bfA); mma(afB, bfB); CORE_IL();
    C::template wait_stages<2>(wid); BAR(); ldfr(NIT - 3, afB, bfB); mma(afA, bfA); CORE_IL();
  }
  C::template wait_stages<1>(wid); BAR(); ldfr(NIT - 2, afA, bfA); mma(afB, bfB); CORE_IL();
  C::template wait_stages<0>(wid); BAR(); ldfr(NIT - 1, afB, bfB); mma(afA, bfA); CORE_IL();
  mma(afB, bfB);
  STAMPK(KID, 2);
}

#define SCHED_IL() do { _Pragma("unroll") for (int _i = 0; _i < 9; ++_i) { __builtin_amdgcn_sched_group_barrier(0x100, 2, 0); __builtin_amdgcn_sched_group_barrier(0x8, 1, 0); } __builtin_amdgcn_sched_group_barrier(0x8, 32, 0); } while (0)

__device__ __forceinline__ bf16x8 tr_frag(const char* p) {
  typedef LDS_AS s16x4* lp;
  s16x4 lo = __builtin_amdgcn_ds_read_tr16_b64_v4i16((lp)(p));
  s16x4 hi = __builtin_amdgcn_ds_read_tr16_b64_v4i16((lp)(p + 4096));
  return __builtin_shufflevector(lo, hi, 0, 1, 2, 3, 4, 5, 6, 7);
}

template <int TYPE>
__device__ __forceinline__ void l1_wg(char* smem, const u16* __restrict__ xn, const u16* __restrict__ w1, const u16* __restrict__ wqkv,
                                      const u16* __restrict__ wf, u16* __restrict__ hbuf, u16* __restrict__ qb, float* __restrict__ kvslab,
                                      int rt, int ct) {
  constexpr int NCH = TYPE ? 38 : 54;
  constexpr int NTW = TYPE ? 5 : 7;
  const int tid = threadIdx.x, lane = tid & 63;
  const int wid = __builtin_amdgcn_readfirstlane(tid >> 6);
  const int wr = wid >> 2, wc = wid & 3, fr = lane & 15, fq = lane >> 4;
  const u16* ag = xn + (size_t)(rt * 128 + wid * 16 + (lane >> 2)) * D + (((lane & 3) ^ ((-(lane >> 4)) & 3)) * 8);
  f32x4 acc[4][NTW];
#pragma unroll
  for (int m = 0; m < 4; ++m)
#pragma unroll
    for (int j = 0; j < NTW; ++j) acc[m][j] = f32x4{0.f, 0.f, 0.f, 0.f};
  auto bsrc = [&](int k, int c, const u16*& p, int& st, int phase) {
    if (TYPE == 0) {
      int gc = ct * 432 + c * 8;
      if (gc < F) { p = w1 + (size_t)k * F + gc; st = 32 * F; }
      else { p = wf + (size_t)k * (2 * D) + (gc - F); st = 32 * 2 * D; }
    } else {
      if (c > 35) c = 35;
      int part = c >= 18 ? 1 : 0, cc = c - 18 * part;
      if (part == 0) { p = wf + (size_t)k * (2 * D) + D + ct * 144 + cc * 8; st = 32 * 2 * D; }
      else { p = wqkv + (size_t)k * (3 * D) + 2 * D + ct * 144 + cc * 8; st = 32 * 3 * D; }
    }
  };
  gemm_core<4, NTW, NCH, 4, 1>(18, smem, ag, bsrc, acc, wr * 64, NTW * wc, 1 << 30, nullptr);
  if (TYPE == 0) {
#pragma unroll
    for (int m = 0; m < 4; ++m)
#pragma unroll
      for (int j = 0; j < NTW; ++j) {
        int T = NTW * wc + j;
        if (T < 27) {
          int row = rt * 128 + wr * 64 + m * 16 + fr;
          int col0 = ct * 432 + T * 16 + fq * 4;
          f32x4 v = acc[m][j];
          if (col0 < F) {
            float o[4];
#pragma unroll
            for (int e = 0; e < 4; ++e) o[e] = v[e] * __builtin_amdgcn_rcpf(1.f + __builtin_amdgcn_exp2f(v[e] * -1.44269504f));
            uint2 pk = make_uint2(pack2(o[0], o[1]), pack2(o[2], o[3]));
            *(uint2*)(hbuf + (size_t)row * F + col0) = pk;
          } else {
            uint2 pk = make_uint2(pack2(v[0], v[1]), pack2(v[2], v[3]));
            *(uint2*)(qb + (size_t)row * D + (col0 - F)) = pk;
          }
        }
      }
  } else {
    constexpr int PK_PITCH = 608, VT_PITCH = 352;
    char* Pk = smem;
    char* Vt = smem + 128 * PK_PITCH;
    __syncthreads();
#pragma unroll
    for (int m = 0; m < 4; ++m)
#pragma unroll
      for (int j = 0; j < NTW; ++j) {
        int T = NTW * wc + j;
        if (T < 18) {
          int row = wr * 64 + m * 16 + fr;
#pragma unroll
          for (int e = 0; e < 4; ++e) {
            int c = T * 16 + fq * 4 + e;
            int part = c >= 144 ? 1 : 0, cc = c - 144 * part, hd = cc / 18, d = cc - hd * 18;
            u16 val = f2bf(acc[m][j][e]);
            if (part == 0) *(u16*)(Pk + row * PK_PITCH + (hd * 36 + d) * 2) = val;
            else *(u16*)(Vt + row * VT_PITCH + (hd * 20 + d) * 2) = val;
          }
        }
      }
    for (int i = tid; i < 128 * 8; i += 512) { int row = i >> 3, hd = i & 7; *(u16*)(Vt + row * VT_PITCH + (hd * 20 + 18) * 2) = 0x3f80; }
    __syncthreads();
    {
      {
        unsigned* pr0 = (unsigned*)(Pk + lane * PK_PITCH + wid * 72);
        unsigned* pr1 = (unsigned*)(Pk + (lane + 64) * PK_PITCH + wid * 72);
        float p0[18], p1[18];
#pragma unroll
        for (int i = 0; i < 9; ++i) {
          unsigned u = pr0[i]; p0[2 * i] = __uint_as_float(u << 16); p0[2 * i + 1] = __uint_as_float(u & 0xffff0000u);
          unsigned w = pr1[i]; p1[2 * i] = __uint_as_float(w << 16); p1[2 * i + 1] = __uint_as_float(w & 0xffff0000u);
        }
        auto finish = [&](float* p, unsigned* pr) {
          float mx = -1e30f, mn = 1e30f;
#pragma unroll
          for (int f = 0; f < 18; ++f) { mx = fmaxf(mx, p[f]); mn = fminf(mn, p[f]); }
          float e1[18], e2[18], s1 = 0.f, s2 = 0.f;
#pragma unroll
          for (int f = 0; f < 18; ++f) { e1[f] = __expf(p[f] - mx); s1 += e1[f]; e2[f] = __expf(mn - p[f]); s2 += e2[f]; }
          float r1 = 1.f / s1, r2 = 1.f / s2;
#pragma unroll
          for (int i = 0; i < 9; ++i) { pr[i] = pack2(e1[2 * i] * r1, e1[2 * i + 1] * r1); pr[9 + i] = pack2(e2[2 * i] * r2, e2[2 * i + 1] * r2); }
        };
        finish(p0, pr0);
        finish(p1, pr1);
      }
    }
    __syncthreads();
    {
      typedef LDS_AS s16x4* lp;
      f32x4 kvacc[3][2];
#pragma unroll
      for (int a = 0; a < 3; ++a) for (int b = 0; b < 2; ++b) kvacc[a][b] = f32x4{0.f, 0.f, 0.f, 0.f};
      const int q = fr >> 2, p = lane & 3;
#pragma unroll
      for (int ks = 0; ks < 4; ++ks) {
        bf16x8 pa[3], vb[2];
        int r0 = ks * 32 + 8 * fq + q;
#pragma unroll
        for (int mt = 0; mt < 3; ++mt) {
          const char* a0 = Pk + r0 * PK_PITCH + (wid * 36 + mt * 16 + 4 * p) * 2;
          s16x4 lo = __builtin_amdgcn_ds_read_tr16_b64_v4i16((lp)(a0));
          s16x4 hi = __builtin_amdgcn_ds_read_tr16_b64_v4i16((lp)(a0 + 4 * PK_PITCH));
          pa[mt] = __builtin_shufflevector(lo, hi, 0, 1, 2, 3, 4, 5, 6, 7);
        }
#pragma unroll
        for (int nt = 0; nt < 2; ++nt) {
          const char* b0 = Vt + r0 * VT_PITCH + (wid * 20 + nt * 16 + 4 * p) * 2;
          s16x4 lo = __builtin_amdgcn_ds_read_tr16_b64_v4i16((lp)(b0));
          s16x4 hi = __builtin_amdgcn_ds_read_tr16_b64_v4i16((lp)(b0 + 4 * VT_PITCH));
          vb[nt] = __builtin_shufflevector(lo, hi, 0, 1, 2, 3, 4, 5, 6, 7);
        }
#pragma unroll
        for (int mt = 0; mt < 3; ++mt)
#pragma unroll
          for (int nt = 0; nt < 2; ++nt) kvacc[mt][nt] = __builtin_amdgcn_mfma_f32_16x16x32_bf16(pa[mt], vb[nt], kvacc[mt][nt], 0, 0, 0);
      }
      float* dst = kvslab + ((size_t)rt * 32 + ct * 8 + wid) * 720;
#pragma unroll
      for (int mt = 0; mt < 3; ++mt)
#pragma unroll
        for (int nt = 0; nt < 2; ++nt)
#pragma unroll
          for (int e = 0; e < 4; ++e) {
            int f = mt * 16 + fq * 4 + e, d = nt * 16 + fr;
            if (f < 36 && d < 19) dst[f * 20 + d] = kvacc[mt][nt][e];
          }
    }
  }
}

__global__ __launch_bounds__(512, 2) void k_l1(const u16* __restrict__ xn, const u16* __restrict__ w1, const u16* __restrict__ wqkv,
                                               const u16* __restrict__ wf, u16* __restrict__ hbuf, u16* __restrict__ qb, float* __restrict__ kvslab) {
  __shared__ __attribute__((aligned(16))) char smem[163840];
  int bid = blockIdx.x, xcd = bid & 7, idx = bid >> 3;
  int rt = (xcd & 1) * 8 + (idx & 7);
  int cs = (xcd >> 1) * 4 + (idx >> 3);
  if (cs < 12) l1_wg<0>(smem, xn, w1, wqkv, wf, hbuf, qb, kvslab, rt, cs);
  else l1_wg<1>(smem, xn, w1, wqkv, wf, hbuf, qb, kvslab, rt, cs - 12);
  STAMPK(1, 3);
}

constexpr int G4_A = 16384, G4_B = 18432, G4_STAGE = G4_A + G4_B;
__global__ __launch_bounds__(512, 2) void k_g2(const u16* __restrict__ hbuf, const u16* __restrict__ w2, const u16* __restrict__ o, const u16* __restrict__ wo,
                                               float* __restrict__ slab) {
  __shared__ __attribute__((aligned(16))) char smem[4 * G4_STAGE];
  int bid = blockIdx.x, xcd = bid & 7, idx = bid >> 3;
  int ks = xcd >> 1, rt = (xcd & 1) * 8 + (idx & 7), ct = idx >> 3;
  const int tid = threadIdx.x, lane = tid & 63, fr = lane & 15, fq = lane >> 4;
  const int wid = __builtin_amdgcn_readfirstlane(tid >> 6), wm = wid & 1, g = (wid >> 1) & 1, wn = wid >> 2;
  f32x4 acc[4][5];
#pragma unroll
  for (int m = 0; m < 4; ++m)
#pragma unroll
    for (int j = 0; j < 5; ++j) acc[m][j] = f32x4{0.f, 0.f, 0.f, 0.f};
  const int nst = ks == 3 ? 21 : 20, nsw = ks == 3 ? 12 : 100;
  const u16* ap[2]; const u16* bp[3];
  STAMPK(2, 0);
  auto setp = [&](const u16* ab, int lda, const u16* bb) {
#pragma unroll
    for (int i = 0; i < 2; ++i) {
      int row = (wid * 2 + i) * 8 + (lane >> 3), lc = (lane & 7) ^ ((row >> 1) & 7);
      ap[i] = ab + (size_t)(rt * 128 + row) * lda + lc * 8;
    }
#pragma unroll
    for (int i = 0; i < 3; ++i) {
      int ci = (wid + 8 * i) * 64 + lane; if (ci > 1151) ci = 1151;
      int rp = ci / 18, c = ci - rp * 18, k = (rp & ~31) | swap23(rp & 31);
      bp[i] = bb + (size_t)k * D + ct * 144 + c * 8;
    }
  };
  setp(hbuf + ks * 1280, F, w2 + (size_t)ks * 1280 * D);
  auto issue = [&](int t) {
    if (t == nsw) setp(o, D, wo);
    char* sb = smem + (t & 3) * G4_STAGE;
#pragma unroll
    for (int i = 0; i < 2; ++i) { GLDS(ap[i], sb + (wid * 2 + i) * 1024); ap[i] += 64; }
#pragma unroll
    for (int i = 0; i < 3; ++i) { if (i < 2 || wid < 2) GLDS(bp[i], sb + G4_A + (wid + 8 * i) * 1024); bp[i] += (size_t)64 * D; }
  };
  const int a_base = (wm * 64 + fr) * 128 + (((g * 4 + fq) ^ ((fr >> 1) & 7)) * 16);
  const int b_base = G4_A + (g * 32 + 16 * (fq >> 1) + 4 * (fq & 1) + (fr >> 2)) * 288 + (10 * wn + ((lane & 3) >> 1)) * 16 + 8 * (lane & 1);
  bf16x8 afA[4], bfA[5], afB[4], bfB[5];
  auto ldfr = [&](int t, bf16x8 (&af)[4], bf16x8 (&bf)[5]) {
    typedef LDS_AS s16x4* lp;
    const char* nb = smem + (t & 3) * G4_STAGE;
#pragma unroll
    for (int j = 0; j < 5; ++j) {
      s16x4 lo = __builtin_amdgcn_ds_read_tr16_b64_v4i16((lp)(nb + b_base + j * 32));
      s16x4 hi = __builtin_amdgcn_ds_read_tr16_b64_v4i16((lp)(nb + b_base + j * 32 + 8 * 288));
      bf[j] = __builtin_shufflevector(lo, hi, 0, 1, 2, 3, 4, 5, 6, 7);
    }
#pragma unroll
    for (int m = 0; m < 4; ++m) af[m] = *(const bf16x8*)(nb + a_base + m * 2048);
  };
  auto mma = [&](bf16x8 (&af)[4], bf16x8 (&bf)[5]) {
#pragma unroll
    for (int j = 0; j < 5; ++j)
      if (j < 4 || wn == 0)
#pragma unroll
      for (int m = 0; m < 4; ++m)
        acc[m][j] = __builtin_amdgcn_mfma_f32_16x16x32_bf16(bf[j], af[m], acc[m][j], 0, 0, 0);
  };
  auto waitst = [&](bool one) {
    if (wid < 2) { if (one) wait_v<5>(); else wait_v<0>(); } else { if (one) wait_v<4>(); else wait_v<0>(); }
  };
#define G4_IL() do { _Pragma("unroll") for (int _i = 0; _i < 14; ++_i) { __builtin_amdgcn_sched_group_barrier(0x100, 1, 0); __builtin_amdgcn_sched_group_barrier(0x8, 1, 0); } __builtin_amdgcn_sched_group_barrier(0x8, 64, 0); } while (0)
  issue(0); issue(1); issue(2);
  if (wid < 2) wait_v<10>(); else wait_v<8>();
  BAR();
  STAMPK(2, 1);
  ldfr(0, afA, bfA);
  int t = 0;
#pragma unroll 1
  for (; t + 2 < nst; t += 2) {
    waitst(t + 3 <= nst); BAR();
    if (t + 3 < nst) issue(t + 3);
    ldfr(t + 1, afB, bfB); mma(afA, bfA); G4_IL();
    waitst(t + 4 <= nst); BAR();
    if (t + 4 < nst) issue(t + 4);
    ldfr(t + 2, afA, bfA); mma(afB, bfB); G4_IL();
  }
  if (t + 1 < nst) {
    wait_v<0>(); BAR();
    ldfr(t + 1, afB, bfB); mma(afA, bfA); G4_IL();
    mma(afB, bfB);
  } else mma(afA, bfA);
  STAMPK(2, 2);
  __syncthreads();
  f32x4* red = (f32x4*)smem;
  const int rs = (wn * 2 + wm) * 20;
  if (g == 1) {
#pragma unroll
    for (int m = 0; m < 4; ++m)
#pragma unroll
      for (int j = 0; j < 5; ++j) red[(rs + m * 5 + j) * 64 + lane] = acc[m][j];
  }
  __syncthreads();
  if (g == 0) {
    float* dst = slab + (size_t)ks * S * D;
#pragma unroll
    for (int m = 0; m < 4; ++m)
#pragma unroll
      for (int j = 0; j < 5; ++j)
        if (wn == 0 || j < 4) *(f32x4*)(dst + (size_t)(rt * 128 + wm * 64 + m * 16 + fr) * D + ct * 144 + (wn * 5 + j) * 16 + fq * 4) = acc[m][j] + red[(rs + m * 5 + j) * 64 + lane];
  }
  STAMPK(2, 3);
}

__global__ __launch_bounds__(256) void k_l3(const float* __restrict__ slab, const u16* __restrict__ x, u16* __restrict__ out) {
  int i = (blockIdx.x * 256 + threadIdx.x) * 4;
  f32x4 v = *(const f32x4*)(slab + i);
#pragma unroll
  for (int k = 1; k < 4; ++k) v += *(const f32x4*)(slab + (size_t)k * S * D + i);
  uint2 xv = *(const uint2*)(x + i);
  float x0 = __uint_as_float(xv.x << 16), x1 = __uint_as_float(xv.x & 0xffff0000u), x2 = __uint_as_float(xv.y << 16), x3 = __uint_as_float(xv.y & 0xffff0000u);
  *(uint2*)(out + i) = make_uint2(pack2(v[0] + x0, v[1] + x1), pack2(v[2] + x2, v[3] + x3));
}

__global__ __launch_bounds__(256) void k_qo3(const u16* __restrict__ pq, const float* __restrict__ kvslab, u16* __restrict__ o) {
  __shared__ __attribute__((aligned(16))) u16 kvT[32 * 72];
  const int h = blockIdx.x >> 3, rg = blockIdx.x & 7, tid = threadIdx.x, lane = tid & 63, wid = tid >> 6;
  const int fr = lane & 15, fq = lane >> 4;
  const int base = fq * 5 - (fq == 3 ? 1 : 0), cnt = fq < 2 ? 5 : 4;
  float p[4][5];
#pragma unroll
  for (int mt = 0; mt < 4; ++mt) {
    const u16* pr = pq + (size_t)(rg * 256 + wid * 64 + mt * 16 + fr) * D + h * DH + base;
#pragma unroll
    for (int j = 0; j < 5; ++j) p[mt][j] = bf2f(pr[j < cnt ? j : 0]);
  }
  {
    float v[3][16];
#pragma unroll
    for (int i = 0; i < 3; ++i) {
      int e = tid + 256 * i; if (e > 683) e = 683;
      int f = e / 19, n = e - f * 19;
#pragma unroll
      for (int r = 0; r < 16; ++r) v[i][r] = kvslab[((size_t)r * 32 + h) * 720 + f * 20 + n];
    }
    for (int i = tid; i < 32 * 36; i += 256) ((unsigned*)kvT)[i] = 0u;
    __syncthreads();
#pragma unroll
    for (int i = 0; i < 3; ++i) {
      int e = tid + 256 * i;
      float t = 0.f;
#pragma unroll
      for (int r = 0; r < 16; ++r) t += v[i][r];
      if (e < 684) {
        int f = e / 19, n = e - f * 19;
        int s = f >= 18 ? 1 : 0, d = f - 18 * s;
        int q = d < 10 ? d / 5 : 2 + (d - 10) / 4;
        int j = d - (q * 5 - (q == 3 ? 1 : 0));
        kvT[n * 72 + s * 32 + q * 8 + j] = f2bf(t);
      }
    }
  }
  __syncthreads();
  bf16x8 bfr[2][2];
#pragma unroll
  for (int s = 0; s < 2; ++s)
#pragma unroll
    for (int j = 0; j < 2; ++j) bfr[s][j] = *(const bf16x8*)(kvT + (j * 16 + fr) * 72 + s * 32 + fq * 8);
#pragma unroll
  for (int mt = 0; mt < 4; ++mt) {
    float mx = p[mt][0], mn = p[mt][0];
#pragma unroll
    for (int j = 1; j < 5; ++j) { mx = fmaxf(mx, p[mt][j]); mn = fminf(mn, p[mt][j]); }
    mx = fmaxf(mx, __shfl_xor(mx, 16)); mx = fmaxf(mx, __shfl_xor(mx, 32));
    mn = fminf(mn, __shfl_xor(mn, 16)); mn = fminf(mn, __shfl_xor(mn, 32));
    float e1[5], e2[5], s1 = 0.f, s2 = 0.f;
#pragma unroll
    for (int j = 0; j < 5; ++j) {
      e1[j] = j < cnt ? __expf(p[mt][j] - mx) : 0.f; e2[j] = j < cnt ? __expf(mn - p[mt][j]) : 0.f;
      s1 += e1[j]; s2 += e2[j];
    }
    s1 += __shfl_xor(s1, 16); s1 += __shfl_xor(s1, 32);
    s2 += __shfl_xor(s2, 16); s2 += __shfl_xor(s2, 32);
    float r1 = 1.f / s1, r2 = 1.f / s2;
    typedef __attribute__((ext_vector_type(4))) unsigned u32x4;
    u32x4 a0 = {pack2(e1[0] * r1, e1[1] * r1), pack2(e1[2] * r1, e1[3] * r1), pack2(e1[4] * r1, 0.f), 0u};
    u32x4 a1 = {pack2(e2[0] * r2, e2[1] * r2), pack2(e2[2] * r2, e2[3] * r2), pack2(e2[4] * r2, 0.f), 0u};
    bf16x8 af0 = __builtin_bit_cast(bf16x8, a0), af1 = __builtin_bit_cast(bf16x8, a1);
    f32x4 acc[2];
#pragma unroll
    for (int j = 0; j < 2; ++j) {
      acc[j] = f32x4{0.f, 0.f, 0.f, 0.f};
      acc[j] = __builtin_amdgcn_mfma_f32_16x16x32_bf16(bfr[0][j], af0, acc[j], 0, 0, 0);
      acc[j] = __builtin_amdgcn_mfma_f32_16x16x32_bf16(bfr[1][j], af1, acc[j], 0, 0, 0);
    }
    float den = rbf(__shfl(acc[1][2], fr)) + 1e-6f;
    float rd = 1.f / den;
    unsigned* dst = (unsigned*)(o + (size_t)(rg * 256 + wid * 64 + mt * 16 + fr) * D + h * DH);
    dst[2 * fq] = pack2(rbf(acc[0][0]) * rd, rbf(acc[0][1]) * rd);
    dst[2 * fq + 1] = pack2(rbf(acc[0][2]) * rd, rbf(acc[0][3]) * rd);
    if (fq == 0) dst[8] = pack2(rbf(acc[1][0]) * rd, rbf(acc[1][1]) * rd);
  }
}

extern "C" void kernel_launch(void* const* d_in, const int* in_sizes, int n_in, void* d_out, int out_size, void* d_ws, size_t ws_size, hipStream_t stream) {
  const u16* x = (const u16*)d_in[0]; const u16* wqkv = (const u16*)d_in[1]; const u16* wphi = (const u16*)d_in[2]; const u16* wo = (const u16*)d_in[3];
  const u16* w1 = (const u16*)d_in[5]; const u16* w2 = (const u16*)d_in[6];
  u16* out = (u16*)d_out;
  char* ws = (char*)d_ws;
  u16* xn = (u16*)ws; ws += (size_t)S * D * 2;
  u16* qb = (u16*)ws; ws += (size_t)S * D * 2;
  u16* hbuf = (u16*)ws; ws += (size_t)S * F * 2;
  float* kvslab = (float*)ws; ws += 16 * 32 * 720 * 4;
  float* kv = (float*)ws; ws += 32 * 720 * 4;
  u16* o = (u16*)ws; ws += (size_t)S * D * 2;
  float* slab = (float*)ws; ws += (size_t)4 * S * D * 4;
  u16* wf = (u16*)ws; ws += (size_t)D * 2 * D * 2;
  k_ln<<<S / 4 + 144 + 128, 256, 0, stream>>>(x, xn, wqkv, wphi, wf, w1, w2);
  k_l1<<<256, 512, 0, stream>>>(xn, w1, wqkv, wf, hbuf, qb, kvslab);
  k_qo3<<<256, 256, 0, stream>>>(qb, kvslab, o);
  k_g2<<<256, 512, 0, stream>>>(hbuf, w2, o, wo, slab);
  k_l3<<<S * D / 1024, 256, 0, stream>>>(slab, x, out);
}
```

```cpp
#include <hip/hip_runtime.h>
#include <hip/hip_bf16.h>
#include <stdint.h>

typedef __attribute__((ext_vector_type(8))) short bf16x8;
typedef __attribute__((ext_vector_type(4))) short s16x4;
typedef __attribute__((ext_vector_type(4))) float f32x4;
typedef unsigned short u16;
#define LDS_AS __attribute__((address_space(3)))

__device__ __forceinline__ float bf2f(u16 v) { return __uint_as_float(((unsigned)v) << 16); }
__device__ __forceinline__ u16 f2bf(float f) {
  unsigned u = __float_as_uint(f);
  u += 0x7fffu + ((u >> 16) & 1u);
  return (u16)(u >> 16);
}
__device__ __forceinline__ unsigned pack2(float lo, float hi) { unsigned r; asm("v_cvt_pk_bf16_f32 %0, %1, %2" : "=v"(r) : "v"(lo), "v"(hi)); return r; }
__device__ __forceinline__ float rbf(float f) { return __uint_as_float(pack2(f, f) << 16); }

constexpr int S = 2048, D = 576, H = 32, DH = 18, F = 4608;

#define STAMPK(k, i)
__global__ __launch_bounds__(256) void k_ln(const u16* __restrict__ x, u16* __restrict__ xn, const u16* __restrict__ wqkv, const u16* __restrict__ wphi, u16* __restrict__ wf,
                                            const u16* __restrict__ w1, const u16* __restrict__ w2, int* __restrict__ cnt) {
  if (blockIdx.x >= S / 4 + 144) {
    int t = (blockIdx.x - S / 4 - 144) * 256 + threadIdx.x;
    const uint4* p1 = (const uint4*)w1; const uint4* p2 = (const uint4*)w2;
    unsigned a = 0;
#pragma unroll
    for (int i = 0; i < 10; ++i) { uint4 v = p1[t + i * 32768 < 331776 ? t + i * 32768 : 0]; uint4 u = p2[t + i * 32768 < 331776 ? t + i * 32768 : 0]; a ^= v.x ^ v.w ^ u.x ^ u.w; }
    if (t < 64) cnt[t] = 0;
    if (a == 0x9e3779b9u) wf[0] = 0;
    return;
  }
  if (blockIdx.x >= S / 4) {
    int u = __builtin_amdgcn_readfirstlane((blockIdx.x - S / 4) * 4 + (threadIdx.x >> 6));
    int wh = u & 63, k = (u >> 6) * 64 + (threadIdx.x & 63), h = wh & 31;
    const u16* src = wqkv + (size_t)k * (3 * D) + wh * 18;
    const unsigned* W = (const unsigned*)(wphi + h * 324);
    float a[18];
#pragma unroll
    for (int i = 0; i < 9; ++i) { unsigned v = *(const unsigned*)(src + 2 * i); a[2 * i] = __uint_as_float(v << 16); a[2 * i + 1] = __uint_as_float(v & 0xffff0000u); }
    u16* dst = wf + (size_t)k * (2 * D) + wh * 18;
#pragma unroll
    for (int f = 0; f < 18; f += 2) {
      float s0 = 0.f, s1 = 0.f;
#pragma unroll
      for (int d = 0; d < 18; ++d) { unsigned w = W[d * 9 + (f >> 1)]; s0 = fmaf(a[d], __uint_as_float(w << 16), s0); s1 = fmaf(a[d], __uint_as_float(w & 0xffff0000u), s1); }
      *(unsigned*)(dst + f) = pack2(s0, s1);
    }
    return;
  }
  int row = blockIdx.x * 4 + (threadIdx.x >> 6), lane = threadIdx.x & 63;
  const u16* xr = x + (size_t)row * D;
  float v[9]; float s = 0.f;
  for (int i = 0; i < 9; ++i) { v[i] = bf2f(xr[lane + 64 * i]); s += v[i]; }
  for (int o = 32; o; o >>= 1) s += __shfl_xor(s, o);
  float m = s * (1.f / D);
  float q = 0.f;
  for (int i = 0; i < 9; ++i) { float d = v[i] - m; q += d * d; }
  for (int o = 32; o; o >>= 1) q += __shfl_xor(q, o);
  float r = rsqrtf(q * (1.f / D) + 1e-5f);
  for (int i = 0; i < 9; ++i) xn[(size_t)row * D + lane + 64 * i] = f2bf((v[i] - m) * r);
}

__device__ __forceinline__ void glds16_asm(const void* gsrc, unsigned lds_dst) {
  unsigned keep;
  asm volatile("s_mov_b32 %0, m0\n\ts_mov_b32 m0, %2\n\ts_nop 0\n\tglobal_load_lds_dwordx4 %1, off\n\ts_mov_b32 m0, %0" : "=&s"(keep) : "v"(gsrc), "s"(lds_dst) : "memory");
}
#define GLDS(g, l) glds16_asm((const void*)(g), (unsigned)(size_t)(l))
#define WAIT_V(n) asm volatile("s_waitcnt vmcnt(" #n ")" ::: "memory")
#define WAIT_L0() asm volatile("s_waitcnt lgkmcnt(0)" ::: "memory")
#define BAR() __builtin_amdgcn_s_barrier()

__device__ __forceinline__ int swap23(int r) { return (r & ~0xC) | ((r & 4) << 1) | ((r & 8) >> 1); }

template <int N> __device__ __forceinline__ void wait_v() { asm volatile("s_waitcnt vmcnt(%0)" :: "n"(N) : "memory"); }
template <int MT, int NTW, int NCH, int NS>
struct Core {
  static constexpr int PITCH = NCH * 16;
  static constexpr int NB = (32 * NCH + 63) / 64;
  static constexpr int STAGE = 8192 + NB * 1024;
  static constexpr int NLO = 1 + NB / 8, NHI = 1 + (NB + 7) / 8, XW = NB % 8;
  template <int K> static __device__ __forceinline__ void wait_stages(int wid) {
    if (XW == 0) wait_v<K * NLO>();
    else { if (wid < XW) wait_v<K * NHI>(); else wait_v<K * NLO>(); }
  }
};

template <int MT, int NTW, int NCH, int NS, int KID, class BSRC>
__device__ __forceinline__ void gemm_core(int NIT, char* smem, const u16* ag0, BSRC&& bsrc, f32x4 (&acc)[MT][NTW], int a_row0  , int tile0  ,
                                          int nsw, const u16* ag1) {
  using C = Core<MT, NTW, NCH, NS>;
  const int tid = threadIdx.x, lane = tid & 63;
  const int wid = __builtin_amdgcn_readfirstlane(tid >> 6);
  const int fr = lane & 15, fq = lane >> 4;
  const u16* ag = ag0;
  const u16* bg[4]; int bst[4];
#pragma unroll
  for (int i = 0; i < 4; ++i) {
    int ci = (wid + 8 * i) * 64 + lane;
    int rho = ci / NCH, c = ci - rho * NCH;
    if (rho > 31) { rho = 31; }
    bsrc(swap23(rho), c, bg[i], bst[i], 0);
  }
  int nissued = 0;
  auto issue = [&](int stg) {
    if (nissued == nsw) {
      ag = ag1;
#pragma unroll
      for (int i = 0; i < 4; ++i) {
        int ci = (wid + 8 * i) * 64 + lane;
        int rho = ci / NCH, c = ci - rho * NCH;
        if (rho > 31) { rho = 31; }
        bsrc(swap23(rho), c, bg[i], bst[i], 1);
      }
    }
    ++nissued;
    char* sb = smem + (stg % NS) * C::STAGE;
    GLDS(ag, sb + wid * 1024); ag += 32;
#pragma unroll
    for (int i = 0; i < 4; ++i) {
      if (8 * i < C::NB && (8 * i + 8 <= C::NB || wid < C::NB - 8 * i)) GLDS(bg[i], sb + 8192 + (wid + 8 * i) * 1024);
      bg[i] += bst[i];
    }
  };
  const int a_base = (a_row0 + fr) * 64 + ((fq ^ ((-(fr >> 2)) & 3)) * 16);
  const int b_base = 8192 + (16 * (fq >> 1) + 4 * (fq & 1) + (fr >> 2)) * C::PITCH + (2 * tile0 + ((lane & 3) >> 1)) * 16 + 8 * (lane & 1);
  bf16x8 afA[MT], bfA[NTW], afB[MT], bfB[NTW];
  auto ldfr = [&](int stg, bf16x8 (&af)[MT], bf16x8 (&bf)[NTW]) {
    typedef LDS_AS s16x4* lp;
    const char* nb = smem + (stg % NS) * C::STAGE;
#pragma unroll
    for (int j = 0; j < NTW; ++j) {
      s16x4 lo = __builtin_amdgcn_ds_read_tr16_b64_v4i16((lp)(nb + b_base + j * 32));
      s16x4 hi = __builtin_amdgcn_ds_read_tr16_b64_v4i16((lp)(nb + b_base + j * 32 + 8 * C::PITCH));
      bf[j] = __builtin_shufflevector(lo, hi, 0, 1, 2, 3, 4, 5, 6, 7);
    }
#pragma unroll
    for (int m = 0; m < MT; ++m) af[m] = *(const bf16x8*)(nb + a_base + m * 1024);
  };
  auto mma = [&](bf16x8 (&af)[MT], bf16x8 (&bf)[NTW]) {
#pragma unroll
    for (int j = 0; j < NTW; ++j)
#pragma unroll
      for (int m = 0; m < MT; ++m)
        acc[m][j] = __builtin_amdgcn_mfma_f32_16x16x32_bf16(bf[j], af[m], acc[m][j], 0, 0, 0);
  };
  constexpr int NRD = 2 * NTW + MT, NMM = MT * NTW;
  constexpr int MPR = NMM / NRD > 0 ? NMM / NRD : 1;
#define CORE_IL() do { _Pragma("unroll") for (int _i = 0; _i < NRD; ++_i) { __builtin_amdgcn_sched_group_barrier(0x100, 1, 0); __builtin_amdgcn_sched_group_barrier(0x8, MPR, 0); } __builtin_amdgcn_sched_group_barrier(0x8, 64, 0); } while (0)
  STAMPK(KID, 0);
#pragma unroll
  for (int s = 0; s < NS - 1; ++s) issue(s);
  C::template wait_stages<NS - 2>(wid);
  BAR();
  STAMPK(KID, 1);
  ldfr(0, afA, bfA);
#pragma unroll 1
  for (int t = 0; t + NS < NIT; t += 2) {
    C::template wait_stages<NS - 3>(wid); BAR();
    issue(t + NS - 1);
    ldfr(t + 1, afB, bfB); mma(afA, bfA); CORE_IL();
    C::template wait_stages<NS - 3>(wid); BAR();
    issue(t + NS);
    ldfr(t + 2, afA, bfA); mma(afB, bfB); CORE_IL();
  }
  static_assert(NS == 4 || NS == 8, "NS");
  C::template wait_stages<NS - 3>(wid); BAR();
  issue(NIT - 1);
  ldfr(NIT - NS + 1, afB, bfB); mma(afA, bfA); CORE_IL();
  if (NS == 8) {
    C::template wait_stages<5>(wid); BAR(); ldfr(NIT - 6, afA, bfA); mma(afB, bfB); CORE_IL();
    C::template wait_stages<4>(wid); BAR(); ldfr(NIT - 5, afB, bfB); mma(afA, bfA); CORE_IL();
    C::template wait_stages<3>(wid); BAR(); ldfr(NIT - 4, afA, bfA); mma(afB, bfB); CORE_IL();
    C::template wait_stages<2>(wid); BAR(); ldfr(NIT - 3, afB, bfB); mma(afA, bfA); CORE_IL();
  }
  C::template wait_stages<1>(wid); BAR(); ldfr(NIT - 2, afA, bfA); mma(afB, bfB); CORE_IL();
  C::template wait_stages<0>(wid); BAR(); ldfr(NIT - 1, afB, bfB); mma(afA, bfA); CORE_IL();
  mma(afB, bfB);
  STAMPK(KID, 2);
}

#define SCHED_IL() do { _Pragma("unroll") for (int _i = 0; _i < 9; ++_i) { __builtin_amdgcn_sched_group_barrier(0x100, 2, 0); __builtin_amdgcn_sched_group_barrier(0x8, 1, 0); } __builtin_amdgcn_sched_group_barrier(0x8, 32, 0); } while (0)

__device__ __forceinline__ bf16x8 tr_frag(const char* p) {
  typedef LDS_AS s16x4* lp;
  s16x4 lo = __builtin_amdgcn_ds_read_tr16_b64_v4i16((lp)(p));
  s16x4 hi = __builtin_amdgcn_ds_read_tr16_b64_v4i16((lp)(p + 4096));
  return __builtin_shufflevector(lo, hi, 0, 1, 2, 3, 4, 5, 6, 7);
}

template <int TYPE>
__device__ __forceinline__ void l1_wg(char* smem, const u16* __restrict__ xn, const u16* __restrict__ w1, const u16* __restrict__ wqkv,
                                      const u16* __restrict__ wf, u16* __restrict__ hbuf, u16* __restrict__ qb, float* __restrict__ kvslab,
                                      int rt, int ct) {
  constexpr int NCH = TYPE ? 38 : 54;
  constexpr int NTW = TYPE ? 5 : 7;
  const int tid = threadIdx.x, lane = tid & 63;
  const int wid = __builtin_amdgcn_readfirstlane(tid >> 6);
  const int wr = wid >> 2, wc = wid & 3, fr = lane & 15, fq = lane >> 4;
  const u16* ag = xn + (size_t)(rt * 128 + wid * 16 + (lane >> 2)) * D + (((lane & 3) ^ ((-(lane >> 4)) & 3)) * 8);
  f32x4 acc[4][NTW];
#pragma unroll
  for (int m = 0; m < 4; ++m)
#pragma unroll
    for (int j = 0; j < NTW; ++j) acc[m][j] = f32x4{0.f, 0.f, 0.f, 0.f};
  auto bsrc = [&](int k, int c, const u16*& p, int& st, int phase) {
    if (TYPE == 0) {
      int gc = ct * 432 + c * 8;
      if (gc < F) { p = w1 + (size_t)k * F + gc; st = 32 * F; }
      else { p = wf + (size_t)k * (2 * D) + (gc - F); st = 32 * 2 * D; }
    } else {
      if (c > 35) c = 35;
      int part = c >= 18 ? 1 : 0, cc = c - 18 * part;
      if (part == 0) { p = wf + (size_t)k * (2 * D) + D + ct * 144 + cc * 8; st = 32 * 2 * D; }
      else { p = wqkv + (size_t)k * (3 * D) + 2 * D + ct * 144 + cc * 8; st = 32 * 3 * D; }
    }
  };
  gemm_core<4, NTW, NCH, 4, 1>(18, smem, ag, bsrc, acc, wr * 64, NTW * wc, 1 << 30, nullptr);
  if (TYPE == 0) {
#pragma unroll
    for (int m = 0; m < 4; ++m)
#pragma unroll
      for (int j = 0; j < NTW; ++j) {
        int T = NTW * wc + j;
        if (T < 27) {
          int row = rt * 128 + wr * 64 + m * 16 + fr;
          int col0 = ct * 432 + T * 16 + fq * 4;
          f32x4 v = acc[m][j];
          if (col0 < F) {
            float o[4];
#pragma unroll
            for (int e = 0; e < 4; ++e) o[e] = v[e] * __builtin_amdgcn_rcpf(1.f + __builtin_amdgcn_exp2f(v[e] * -1.44269504f));
            uint2 pk = make_uint2(pack2(o[0], o[1]), pack2(o[2], o[3]));
            *(uint2*)(hbuf + (size_t)row * F + col0) = pk;
          } else {
            uint2 pk = make_uint2(pack2(v[0], v[1]), pack2(v[2], v[3]));
            *(uint2*)(qb + (size_t)row * D + (col0 - F)) = pk;
          }
        }
      }
  } else {
    constexpr int PK_PITCH = 608, VT_PITCH = 352;
    char* Pk = smem;
    char* Vt = smem + 128 * PK_PITCH;
    __syncthreads();
#pragma unroll
    for (int m = 0; m < 4; ++m)
#pragma unroll
      for (int j = 0; j < NTW; ++j) {
        int T = NTW * wc + j;
        if (T < 18) {
          int row = wr * 64 + m * 16 + fr;
#pragma unroll
          for (int e = 0; e < 4; ++e) {
            int c = T * 16 + fq * 4 + e;
            int part = c >= 144 ? 1 : 0, cc = c - 144 * part, hd = cc / 18, d = cc - hd * 18;
            u16 val = f2bf(acc[m][j][e]);
            if (part == 0) *(u16*)(Pk + row * PK_PITCH + (hd * 36 + d) * 2) = val;
            else *(u16*)(Vt + row * VT_PITCH + (hd * 20 + d) * 2) = val;
          }
        }
      }
    for (int i = tid; i < 128 * 8; i += 512) { int row = i >> 3, hd = i & 7; *(u16*)(Vt + row * VT_PITCH + (hd * 20 + 18) * 2) = 0x3f80; }
    __syncthreads();
    {
      {
        unsigned* pr0 = (unsigned*)(Pk + lane * PK_PITCH + wid * 72);
        unsigned* pr1 = (unsigned*)(Pk + (lane + 64) * PK_PITCH + wid * 72);
        float p0[18], p1[18];
#pragma unroll
        for (int i = 0; i < 9; ++i) {
          unsigned u = pr0[i]; p0[2 * i] = __uint_as_float(u << 16); p0[2 * i + 1] = __uint_as_float(u & 0xffff0000u);
          unsigned w = pr1[i]; p1[2 * i] = __uint_as_float(w << 16); p1[2 * i + 1] = __uint_as_float(w & 0xffff0000u);
        }
        auto finish = [&](float* p, unsigned* pr) {
          float mx = -1e30f, mn = 1e30f;
#pragma unroll
          for (int f = 0; f < 18; ++f) { mx = fmaxf(mx, p[f]); mn = fminf(mn, p[f]); }
          float e1[18], e2[18], s1 = 0.f, s2 = 0.f;
#pragma unroll
          for (int f = 0; f < 18; ++f) { e1[f] = __expf(p[f] - mx); s1 += e1[f]; e2[f] = __expf(mn - p[f]); s2 += e2[f]; }
          float r1 = 1.f / s1, r2 = 1.f / s2;
#pragma unroll
          for (int i = 0; i < 9; ++i) { pr[i] = pack2(e1[2 * i] * r1, e1[2 * i + 1] * r1); pr[9 + i] = pack2(e2[2 * i] * r2, e2[2 * i + 1] * r2); }
        };
        finish(p0, pr0);
        finish(p1, pr1);
      }
    }
    __syncthreads();
    {
      typedef LDS_AS s16x4* lp;
      f32x4 kvacc[3][2];
#pragma unroll
      for (int a = 0; a < 3; ++a) for (int b = 0; b < 2; ++b) kvacc[a][b] = f32x4{0.f, 0.f, 0.f, 0.f};
      const int q = fr >> 2, p = lane & 3;
#pragma unroll
      for (int ks = 0; ks < 4; ++ks) {
        bf16x8 pa[3], vb[2];
        int r0 = ks * 32 + 8 * fq + q;
#pragma unroll
        for (int mt = 0; mt < 3; ++mt) {
          const char* a0 = Pk + r0 * PK_PITCH + (wid * 36 + mt * 16 + 4 * p) * 2;
          s16x4 lo = __builtin_amdgcn_ds_read_tr16_b64_v4i16((lp)(a0));
          s16x4 hi = __builtin_amdgcn_ds_read_tr16_b64_v4i16((lp)(a0 + 4 * PK_PITCH));
          pa[mt] = __builtin_shufflevector(lo, hi, 0, 1, 2, 3, 4, 5, 6, 7);
        }
#pragma unroll
        for (int nt = 0; nt < 2; ++nt) {
          const char* b0 = Vt + r0 * VT_PITCH + (wid * 20 + nt * 16 + 4 * p) * 2;
          s16x4 lo = __builtin_amdgcn_ds_read_tr16_b64_v4i16((lp)(b0));
          s16x4 hi = __builtin_amdgcn_ds_read_tr16_b64_v4i16((lp)(b0 + 4 * VT_PITCH));
          vb[nt] = __builtin_shufflevector(lo, hi, 0, 1, 2, 3, 4, 5, 6, 7);
        }
#pragma unroll
        for (int mt = 0; mt < 3; ++mt)
#pragma unroll
          for (int nt = 0; nt < 2; ++nt) kvacc[mt][nt] = __builtin_amdgcn_mfma_f32_16x16x32_bf16(pa[mt], vb[nt], kvacc[mt][nt], 0, 0, 0);
      }
      float* dst = kvslab + ((size_t)rt * 32 + ct * 8 + wid) * 720;
#pragma unroll
      for (int mt = 0; mt < 3; ++mt)
#pragma unroll
        for (int nt = 0; nt < 2; ++nt)
#pragma unroll
          for (int e = 0; e < 4; ++e) {
            int f = mt * 16 + fq * 4 + e, d = nt * 16 + fr;
            if (f < 36 && d < 19) dst[f * 20 + d] = kvacc[mt][nt][e];
          }
    }
  }
}

__global__ __launch_bounds__(512, 2) void k_l1(const u16* __restrict__ xn, const u16* __restrict__ w1, const u16* __restrict__ wqkv,
                                               const u16* __restrict__ wf, u16* __restrict__ hbuf, u16* __restrict__ qb, float* __restrict__ kvslab) {
  __shared__ __attribute__((aligned(16))) char smem[163840];
  int bid = blockIdx.x, xcd = bid & 7, idx = bid >> 3;
  int rt = (xcd & 1) * 8 + (idx & 7);
  int cs = (xcd >> 1) * 4 + (idx >> 3);
  if (cs < 12) l1_wg<0>(smem, xn, w1, wqkv, wf, hbuf, qb, kvslab, rt, cs);
  else l1_wg<1>(smem, xn, w1, wqkv, wf, hbuf, qb, kvslab, rt, cs - 12);
  STAMPK(1, 3);
}

constexpr int G4_A = 16384, G4_B = 18432, G4_STAGE = G4_A + G4_B;
__global__ __launch_bounds__(512, 2) void k_g2(const u16* __restrict__ hbuf, const u16* __restrict__ w2, const u16* __restrict__ o, const u16* __restrict__ wo,
                                               float* __restrict__ slab, int* __restrict__ cnt, const u16* __restrict__ x, u16* __restrict__ out) {
  __shared__ __attribute__((aligned(16))) char smem[4 * G4_STAGE];
  int bid = blockIdx.x, xcd = bid & 7, idx = bid >> 3;
  int ks = xcd >> 1, rt = (xcd & 1) * 8 + (idx & 7), ct = idx >> 3;
  const int tid = threadIdx.x, lane = tid & 63, fr = lane & 15, fq = lane >> 4;
  const int wid = __builtin_amdgcn_readfirstlane(tid >> 6), wm = wid & 1, g = (wid >> 1) & 1, wn = wid >> 2;
  f32x4 acc[4][5];
#pragma unroll
  for (int m = 0; m < 4; ++m)
#pragma unroll
    for (int j = 0; j < 5; ++j) acc[m][j] = f32x4{0.f, 0.f, 0.f, 0.f};
  const int nst = ks == 3 ? 21 : 20, nsw = ks == 3 ? 12 : 100;
  const u16* ap[2]; const u16* bp[3];
  STAMPK(2, 0);
  auto setp = [&](const u16* ab, int lda, const u16* bb) {
#pragma unroll
    for (int i = 0; i < 2; ++i) {
      int row = (wid * 2 + i) * 8 + (lane >> 3), lc = (lane & 7) ^ ((row >> 1) & 7);
      ap[i] = ab + (size_t)(rt * 128 + row) * lda + lc * 8;
    }
#pragma unroll
    for (int i = 0; i < 3; ++i) {
      int ci = (wid + 8 * i) * 64 + lane; if (ci > 1151) ci = 1151;
      int rp = ci / 18, c = ci - rp * 18, k = (rp & ~31) | swap23(rp & 31);
      bp[i] = bb + (size_t)k * D + ct * 144 + c * 8;
    }
  };
  setp(hbuf + ks * 1280, F, w2 + (size_t)ks * 1280 * D);
  auto issue = [&](int t) {
    if (t == nsw) setp(o, D, wo);
    char* sb = smem + (t & 3) * G4_STAGE;
#pragma unroll
    for (int i = 0; i < 2; ++i) { GLDS(ap[i], sb + (wid * 2 + i) * 1024); ap[i] += 64; }
#pragma unroll
    for (int i = 0; i < 3; ++i) { if (i < 2 || wid < 2) GLDS(bp[i], sb + G4_A + (wid + 8 * i) * 1024); bp[i] += (size_t)64 * D; }
  };
  const int a_base = (wm * 64 + fr) * 128 + (((g * 4 + fq) ^ ((fr >> 1) & 7)) * 16);
  const int b_base = G4_A + (g * 32 + 16 * (fq >> 1) + 4 * (fq & 1) + (fr >> 2)) * 288 + (10 * wn + ((lane & 3) >> 1)) * 16 + 8 * (lane & 1);
  bf16x8 afA[4], bfA[5], afB[4], bfB[5];
  auto ldfr = [&](int t, bf16x8 (&af)[4], bf16x8 (&bf)[5]) {
    typedef LDS_AS s16x4* lp;
    const char* nb = smem + (t & 3) * G4_STAGE;
#pragma unroll
    for (int j = 0; j < 5; ++j) {
      s16x4 lo = __builtin_amdgcn_ds_read_tr16_b64_v4i16((lp)(nb + b_base + j * 32));
      s16x4 hi = __builtin_amdgcn_ds_read_tr16_b64_v4i16((lp)(nb + b_base + j * 32 + 8 * 288));
      bf[j] = __builtin_shufflevector(lo, hi, 0, 1, 2, 3, 4, 5, 6, 7);
    }
#pragma unroll
    for (int m = 0; m < 4; ++m) af[m] = *(const bf16x8*)(nb + a_base + m * 2048);
  };
  auto mma = [&](bf16x8 (&af)[4], bf16x8 (&bf)[5]) {
#pragma unroll
    for (int j = 0; j < 5; ++j)
      if (j < 4 || wn == 0)
#pragma unroll
      for (int m = 0; m < 4; ++m)
        acc[m][j] = __builtin_amdgcn_mfma_f32_16x16x32_bf16(bf[j], af[m], acc[m][j], 0, 0, 0);
  };
  auto waitst = [&](bool one) {
    if (wid < 2) { if (one) wait_v<5>(); else wait_v<0>(); } else { if (one) wait_v<4>(); else wait_v<0>(); }
  };
#define G4_IL() do { _Pragma("unroll") for (int _i = 0; _i < 14; ++_i) { __builtin_amdgcn_sched_group_barrier(0x100, 1, 0); __builtin_amdgcn_sched_group_barrier(0x8, 1, 0); } __builtin_amdgcn_sched_group_barrier(0x8, 64, 0); } while (0)
  issue(0); issue(1); issue(2);
  if (wid < 2) wait_v<10>(); else wait_v<8>();
  BAR();
  STAMPK(2, 1);
  ldfr(0, afA, bfA);
  int t = 0;
#pragma unroll 1
  for (; t + 2 < nst; t += 2) {
    waitst(t + 3 <= nst); BAR();
    if (t + 3 < nst) issue(t + 3);
    ldfr(t + 1, afB, bfB); mma(afA, bfA); G4_IL();
    waitst(t + 4 <= nst); BAR();
    if (t + 4 < nst) issue(t + 4);
    ldfr(t + 2, afA, bfA); mma(afB, bfB); G4_IL();
  }
  if (t + 1 < nst) {
    wait_v<0>(); BAR();
    ldfr(t + 1, afB, bfB); mma(afA, bfA); G4_IL();
    mma(afB, bfB);
  } else mma(afA, bfA);
  STAMPK(2, 2);
  __syncthreads();
  f32x4* red = (f32x4*)smem;
  const int rs = (wn * 2 + wm) * 20;
  if (g == 1) {
#pragma unroll
    for (int m = 0; m < 4; ++m)
#pragma unroll
      for (int j = 0; j < 5; ++j) red[(rs + m * 5 + j) * 64 + lane] = acc[m][j];
  }
  __syncthreads();
  if (g == 0) {
    float* dst = slab + (size_t)ks * S * D;
#pragma unroll
    for (int m = 0; m < 4; ++m)
#pragma unroll
      for (int j = 0; j < 5; ++j)
        if (wn == 0 || j < 4) *(f32x4*)(dst + (size_t)(rt * 128 + wm * 64 + m * 16 + fr) * D + ct * 144 + (wn * 5 + j) * 16 + fq * 4) = acc[m][j] + red[(rs + m * 5 + j) * 64 + lane];
    __threadfence();
  }
  __syncthreads();
  int* flag = (int*)smem;
  if (tid == 0) flag[0] = atomicAdd(cnt + rt * 4 + ct, 1);
  __syncthreads();
  if (flag[0] == 3) {
    __threadfence();
#pragma unroll 3
    for (int i = 0; i < 9; ++i) {
      int e = (i * 512 + tid) * 4, r = e / 144, c = e - r * 144;
      size_t off = (size_t)(rt * 128 + r) * D + ct * 144 + c;
      f32x4 v = *(const f32x4*)(slab + off);
#pragma unroll
      for (int k = 1; k < 4; ++k) v += *(const f32x4*)(slab + (size_t)k * S * D + off);
      uint2 xv = *(const uint2*)(x + off);
      float x0 = __uint_as_float(xv.x << 16), x1 = __uint_as_float(xv.x & 0xffff0000u), x2 = __uint_as_float(xv.y << 16), x3 = __uint_as_float(xv.y & 0xffff0000u);
      *(uint2*)(out + off) = make_uint2(pack2(v[0] + x0, v[1] + x1), pack2(v[2] + x2, v[3] + x3));
    }
  }
  STAMPK(2, 3);
}

__global__ __launch_bounds__(256) void k_l3(const float* __restrict__ slab, const u16* __restrict__ x, u16* __restrict__ out) {
  int i = (blockIdx.x * 256 + threadIdx.x) * 4;
  f32x4 v = *(const f32x4*)(slab + i);
#pragma unroll
  for (int k = 1; k < 4; ++k) v += *(const f32x4*)(slab + (size_t)k * S * D + i);
  uint2 xv = *(const uint2*)(x + i);
  float x0 = __uint_as_float(xv.x << 16), x1 = __uint_as_float(xv.x & 0xffff0000u), x2 = __uint_as_float(xv.y << 16), x3 = __uint_as_float(xv.y & 0xffff0000u);
  *(uint2*)(out + i) = make_uint2(pack2(v[0] + x0, v[1] + x1), pack2(v[2] + x2, v[3] + x3));
}

__global__ __launch_bounds__(256) void k_qo3(const u16* __restrict__ pq, const float* __restrict__ kvslab, u16* __restrict__ o) {
  __shared__ __attribute__((aligned(16))) u16 kvT[32 * 72];
  const int h = blockIdx.x >> 3, rg = blockIdx.x & 7, tid = threadIdx.x, lane = tid & 63, wid = tid >> 6;
  const int fr = lane & 15, fq = lane >> 4;
  const int base = fq * 5 - (fq == 3 ? 1 : 0), cnt = fq < 2 ? 5 : 4;
  float p[4][5];
#pragma unroll
  for (int mt = 0; mt < 4; ++mt) {
    const u16* pr = pq + (size_t)(rg * 256 + wid * 64 + mt * 16 + fr) * D + h * DH + base;
#pragma unroll
    for (int j = 0; j < 5; ++j) p[mt][j] = bf2f(pr[j < cnt ? j : 0]);
  }
  {
    float v[3][16];
#pragma unroll
    for (int i = 0; i < 3; ++i) {
      int e = tid + 256 * i; if (e > 683) e = 683;
      int f = e / 19, n = e - f * 19;
#pragma unroll
      for (int r = 0; r < 16; ++r) v[i][r] = kvslab[((size_t)r * 32 + h) * 720 + f * 20 + n];
    }
    for (int i = tid; i < 32 * 36; i += 256) ((unsigned*)kvT)[i] = 0u;
    __syncthreads();
#pragma unroll
    for (int i = 0; i < 3; ++i) {
      int e = tid + 256 * i;
      float t = 0.f;
#pragma unroll
      for (int r = 0; r < 16; ++r) t += v[i][r];
      if (e < 684) {
        int f = e / 19, n = e - f * 19;
        int s = f >= 18 ? 1 : 0, d = f - 18 * s;
        int q = d < 10 ? d / 5 : 2 + (d - 10) / 4;
        int j = d - (q * 5 - (q == 3 ? 1 : 0));
        kvT[n * 72 + s * 32 + q * 8 + j] = f2bf(t);
      }
    }
  }
  __syncthreads();
  bf16x8 bfr[2][2];
#pragma unroll
  for (int s = 0; s < 2; ++s)
#pragma unroll
    for (int j = 0; j < 2; ++j) bfr[s][j] = *(const bf16x8*)(kvT + (j * 16 + fr) * 72 + s * 32 + fq * 8);
#pragma unroll
  for (int mt = 0; mt < 4; ++mt) {
    float mx = p[mt][0], mn = p[mt][0];
#pragma unroll
    for (int j = 1; j < 5; ++j) { mx = fmaxf(mx, p[mt][j]); mn = fminf(mn, p[mt][j]); }
    mx = fmaxf(mx, __shfl_xor(mx, 16)); mx = fmaxf(mx, __shfl_xor(mx, 32));
    mn = fminf(mn, __shfl_xor(mn, 16)); mn = fminf(mn, __shfl_xor(mn, 32));
    float e1[5], e2[5], s1 = 0.f, s2 = 0.f;
#pragma unroll
    for (int j = 0; j < 5; ++j) {
      e1[j] = j < cnt ? __expf(p[mt][j] - mx) : 0.f; e2[j] = j < cnt ? __expf(mn - p[mt][j]) : 0.f;
      s1 += e1[j]; s2 += e2[j];
    }
    s1 += __shfl_xor(s1, 16); s1 += __shfl_xor(s1, 32);
    s2 += __shfl_xor(s2, 16); s2 += __shfl_xor(s2, 32);
    float r1 = 1.f / s1, r2 = 1.f / s2;
    typedef __attribute__((ext_vector_type(4))) unsigned u32x4;
    u32x4 a0 = {pack2(e1[0] * r1, e1[1] * r1), pack2(e1[2] * r1, e1[3] * r1), pack2(e1[4] * r1, 0.f), 0u};
    u32x4 a1 = {pack2(e2[0] * r2, e2[1] * r2), pack2(e2[2] * r2, e2[3] * r2), pack2(e2[4] * r2, 0.f), 0u};
    bf16x8 af0 = __builtin_bit_cast(bf16x8, a0), af1 = __builtin_bit_cast(bf16x8, a1);
    f32x4 acc[2];
#pragma unroll
    for (int j = 0; j < 2; ++j) {
      acc[j] = f32x4{0.f, 0.f, 0.f, 0.f};
      acc[j] = __builtin_amdgcn_mfma_f32_16x16x32_bf16(bfr[0][j], af0, acc[j], 0, 0, 0);
      acc[j] = __builtin_amdgcn_mfma_f32_16x16x32_bf16(bfr[1][j], af1, acc[j], 0, 0, 0);
    }
    float den = rbf(__shfl(acc[1][2], fr)) + 1e-6f;
    float rd = 1.f / den;
    unsigned* dst = (unsigned*)(o + (size_t)(rg * 256 + wid * 64 + mt * 16 + fr) * D + h * DH);
    dst[2 * fq] = pack2(rbf(acc[0][0]) * rd, rbf(acc[0][1]) * rd);
    dst[2 * fq + 1] = pack2(rbf(acc[0][2]) * rd, rbf(acc[0][3]) * rd);
    if (fq == 0) dst[8] = pack2(rbf(acc[1][0]) * rd, rbf(acc[1][1]) * rd);
  }
}

extern "C" void kernel_launch(void* const* d_in, const int* in_sizes, int n_in, void* d_out, int out_size, void* d_ws, size_t ws_size, hipStream_t stream) {
  const u16* x = (const u16*)d_in[0]; const u16* wqkv = (const u16*)d_in[1]; const u16* wphi = (const u16*)d_in[2]; const u16* wo = (const u16*)d_in[3];
  const u16* w1 = (const u16*)d_in[5]; const u16* w2 = (const u16*)d_in[6];
  u16* out = (u16*)d_out;
  char* ws = (char*)d_ws;
  u16* xn = (u16*)ws; ws += (size_t)S * D * 2;
  u16* qb = (u16*)ws; ws += (size_t)S * D * 2;
  u16* hbuf = (u16*)ws; ws += (size_t)S * F * 2;
  float* kvslab = (float*)ws; ws += 16 * 32 * 720 * 4;
  float* kv = (float*)ws; ws += 32 * 720 * 4;
  u16* o = (u16*)ws; ws += (size_t)S * D * 2;
  float* slab = (float*)ws; ws += (size_t)4 * S * D * 4;
  u16* wf = (u16*)ws; ws += (size_t)D * 2 * D * 2;
  int* cnt = (int*)ws; ws += 256;
  k_ln<<<S / 4 + 144 + 128, 256, 0, stream>>>(x, xn, wqkv, wphi, wf, w1, w2, cnt);
  k_l1<<<256, 512, 0, stream>>>(xn, w1, wqkv, wf, hbuf, qb, kvslab);
  k_qo3<<<256, 256, 0, stream>>>(qb, kvslab, o);
  k_g2<<<256, 512, 0, stream>>>(hbuf, w2, o, wo, slab, cnt, x, out);
}
```

```cpp
#include <hip/hip_runtime.h>
#include <hip/hip_bf16.h>
#include <stdint.h>

typedef __attribute__((ext_vector_type(8))) short bf16x8;
typedef __attribute__((ext_vector_type(4))) short s16x4;
typedef __attribute__((ext_vector_type(4))) float f32x4;
typedef unsigned short u16;
#define LDS_AS __attribute__((address_space(3)))

__device__ __forceinline__ float bf2f(u16 v) { return __uint_as_float(((unsigned)v) << 16); }
__device__ __forceinline__ u16 f2bf(float f) {
  unsigned u = __float_as_uint(f);
  u += 0x7fffu + ((u >> 16) & 1u);
  return (u16)(u >> 16);
}
__device__ __forceinline__ unsigned pack2(float lo, float hi) { unsigned r; asm("v_cvt_pk_bf16_f32 %0, %1, %2" : "=v"(r) : "v"(lo), "v"(hi)); return r; }
__device__ __forceinline__ float rbf(float f) { return __uint_as_float(pack2(f, f) << 16); }

constexpr int S = 2048, D = 576, H = 32, DH = 18, F = 4608;

#define STAMPK(k, i)
__global__ __launch_bounds__(256) void k_ln(const u16* __restrict__ x, u16* __restrict__ xn, const u16* __restrict__ wqkv, const u16* __restrict__ wphi, u16* __restrict__ wf,
                                            const u16* __restrict__ w1, const u16* __restrict__ w2) {
  if (blockIdx.x >= S / 4 + 144) {
    int t = (blockIdx.x - S / 4 - 144) * 256 + threadIdx.x;
    const uint4* p1 = (const uint4*)w1; const uint4* p2 = (const uint4*)w2;
    unsigned a = 0;
#pragma unroll
    for (int i = 0; i < 10; ++i) { uint4 v = p1[t + i * 32768 < 331776 ? t + i * 32768 : 0]; uint4 u = p2[t + i * 32768 < 331776 ? t + i * 32768 : 0]; a ^= v.x ^ v.w ^ u.x ^ u.w; }
    if (a == 0x9e3779b9u) wf[0] = 0;
    return;
  }
  if (blockIdx.x >= S / 4) {
    int u = __builtin_amdgcn_readfirstlane((blockIdx.x - S / 4) * 4 + (threadIdx.x >> 6));
    int wh = u & 63, k = (u >> 6) * 64 + (threadIdx.x & 63), h = wh & 31;
    const u16* src = wqkv + (size_t)k * (3 * D) + wh * 18;
    const unsigned* W = (const unsigned*)(wphi + h * 324);
    float a[18];
#pragma unroll
    for (int i = 0; i < 9; ++i) { unsigned v = *(const unsigned*)(src + 2 * i); a[2 * i] = __uint_as_float(v << 16); a[2 * i + 1] = __uint_as_float(v & 0xffff0000u); }
    u16* dst = wf + (size_t)k * (2 * D) + wh * 18;
#pragma unroll
    for (int f = 0; f < 18; f += 2) {
      float s0 = 0.f, s1 = 0.f;
#pragma unroll
      for (int d = 0; d < 18; ++d) { unsigned w = W[d * 9 + (f >> 1)]; s0 = fmaf(a[d], __uint_as_float(w << 16), s0); s1 = fmaf(a[d], __uint_as_float(w & 0xffff0000u), s1); }
      *(unsigned*)(dst + f) = pack2(s0, s1);
    }
    return;
  }
  int row = blockIdx.x * 4 + (threadIdx.x >> 6), lane = threadIdx.x & 63;
  const u16* xr = x + (size_t)row * D;
  float v[9]; float s = 0.f;
  for (int i = 0; i < 9; ++i) { v[i] = bf2f(xr[lane + 64 * i]); s += v[i]; }
  for (int o = 32; o; o >>= 1) s += __shfl_xor(s, o);
  float m = s * (1.f / D);
  float q = 0.f;
  for (int i = 0; i < 9; ++i) { float d = v[i] - m; q += d * d; }
  for (int o = 32; o; o >>= 1) q += __shfl_xor(q, o);
  float r = rsqrtf(q * (1.f / D) + 1e-5f);
  for (int i = 0; i < 9; ++i) xn[(size_t)row * D + lane + 64 * i] = f2bf((v[i] - m) * r);
}

__device__ __forceinline__ void glds16_asm(const void* gsrc, unsigned lds_dst) {
  unsigned keep;
  asm volatile("s_mov_b32 %0, m0\n\ts_mov_b32 m0, %2\n\ts_nop 0\n\tglobal_load_lds_dwordx4 %1, off\n\ts_mov_b32 m0, %0" : "=&s"(keep) : "v"(gsrc), "s"(lds_dst) : "memory");
}
#define GLDS(g, l) glds16_asm((const void*)(g), (unsigned)(size_t)(l))
#define WAIT_V(n) asm volatile("s_waitcnt vmcnt(" #n ")" ::: "memory")
#define WAIT_L0() asm volatile("s_waitcnt lgkmcnt(0)" ::: "memory")
#define BAR() __builtin_amdgcn_s_barrier()

__device__ __forceinline__ int swap23(int r) { return (r & ~0xC) | ((r & 4) << 1) | ((r & 8) >> 1); }

template <int N> __device__ __forceinline__ void wait_v() { asm volatile("s_waitcnt vmcnt(%0)" :: "n"(N) : "memory"); }
template <int MT, int NTW, int NCH, int NS>
struct Core {
  static constexpr int PITCH = NCH * 16;
  static constexpr int NB = (32 * NCH + 63) / 64;
  static constexpr int STAGE = 8192 + NB * 1024;
  static constexpr int NLO = 1 + NB / 8, NHI = 1 + (NB + 7) / 8, XW = NB % 8;
  template <int K> static __device__ __forceinline__ void wait_stages(int wid) {
    if (XW == 0) wait_v<K * NLO>();
    else { if (wid < XW) wait_v<K * NHI>(); else wait_v<K * NLO>(); }
  }
};

template <int MT, int NTW, int NCH, int NS, int KID, class BSRC>
__device__ __forceinline__ void gemm_core(int NIT, char* smem, const u16* ag0, BSRC&& bsrc, f32x4 (&acc)[MT][NTW], int a_row0  , int tile0  ,
                                          int nsw, const u16* ag1) {
  using C = Core<MT, NTW, NCH, NS>;
  const int tid = threadIdx.x, lane = tid & 63;
  const int wid = __builtin_amdgcn_readfirstlane(tid >> 6);
  const int fr = lane & 15, fq = lane >> 4;
  const u16* ag = ag0;
  const u16* bg[4]; int bst[4];
#pragma unroll
  for (int i = 0; i < 4; ++i) {
    int ci = (wid + 8 * i) * 64 + lane;
    int rho = ci / NCH, c = ci - rho * NCH;
    if (rho > 31) { rho = 31; }
    bsrc(swap23(rho), c, bg[i], bst[i], 0);
  }
  int nissued = 0;
  auto issue = [&](int stg) {
    if (nissued == nsw) {
      ag = ag1;
#pragma unroll
      for (int i = 0; i < 4; ++i) {
        int ci = (wid + 8 * i) * 64 + lane;
        int rho = ci / NCH, c = ci - rho * NCH;
        if (rho > 31) { rho = 31; }
        bsrc(swap23(rho), c, bg[i], bst[i], 1);
      }
    }
    ++nissued;
    char* sb = smem + (stg % NS) * C::STAGE;
    GLDS(ag, sb + wid * 1024); ag += 32;
#pragma unroll
    for (int i = 0; i < 4; ++i) {
      if (8 * i < C::NB && (8 * i + 8 <= C::NB || wid < C::NB - 8 * i)) GLDS(bg[i], sb + 8192 + (wid + 8 * i) * 1024);
      bg[i] += bst[i];
    }
  };
  const int a_base = (a_row0 + fr) * 64 + ((fq ^ ((-(fr >> 2)) & 3)) * 16);
  const int b_base = 8192 + (16 * (fq >> 1) + 4 * (fq & 1) + (fr >> 2)) * C::PITCH + (2 * tile0 + ((lane & 3) >> 1)) * 16 + 8 * (lane & 1);
  bf16x8 afA[MT], bfA[NTW], afB[MT], bfB[NTW];
  auto ldfr = [&](int stg, bf16x8 (&af)[MT], bf16x8 (&bf)[NTW]) {
    typedef LDS_AS s16x4* lp;
    const char* nb = smem + (stg % NS) * C::STAGE;
#pragma unroll
    for (int j = 0; j < NTW; ++j) {
      s16x4 lo = __builtin_amdgcn_ds_read_tr16_b64_v4i16((lp)(nb + b_base + j * 32));
      s16x4 hi = __builtin_amdgcn_ds_read_tr16_b64_v4i16((lp)(nb + b_base + j * 32 + 8 * C::PITCH));
      bf[j] = __builtin_shufflevector(lo, hi, 0, 1, 2, 3, 4, 5, 6, 7);
    }
#pragma unroll
    for (int m = 0; m < MT; ++m) af[m] = *(const bf16x8*)(nb + a_base + m * 1024);
  };
  auto mma = [&](bf16x8 (&af)[MT], bf16x8 (&bf)[NTW]) {
#pragma unroll
    for (int j = 0; j < NTW; ++j)
#pragma unroll
      for (int m = 0; m < MT; ++m)
        acc[m][j] = __builtin_amdgcn_mfma_f32_16x16x32_bf16(bf[j], af[m], acc[m][j], 0, 0, 0);
  };
  constexpr int NRD = 2 * NTW + MT, NMM = MT * NTW;
  constexpr int MPR = NMM / NRD > 0 ? NMM / NRD : 1;
#define CORE_IL() do { _Pragma("unroll") for (int _i = 0; _i < NRD; ++_i) { __builtin_amdgcn_sched_group_barrier(0x100, 1, 0); __builtin_amdgcn_sched_group_barrier(0x8, MPR, 0); } __builtin_amdgcn_sched_group_barrier(0x8, 64, 0); } while (0)
  STAMPK(KID, 0);
#pragma unroll
  for (int s = 0; s < NS - 1; ++s) issue(s);
  C::template wait_stages<NS - 2>(wid);
  BAR();
  STAMPK(KID, 1);
  ldfr(0, afA, bfA);
#pragma unroll 1
  for (int t = 0; t + NS < NIT; t += 2) {
    C::template wait_stages<NS - 3>(wid); BAR();
    issue(t + NS - 1);
    ldfr(t + 1, afB, bfB); mma(afA, bfA); CORE_IL();
    C::template wait_stages<NS - 3>(wid); BAR();
    issue(t + NS);
    ldfr(t + 2, afA, bfA); mma(afB, bfB); CORE_IL();
  }
  static_assert(NS == 4 || NS == 8, "NS");
  C::template wait_stages<NS - 3>(wid); BAR();
  issue(NIT - 1);
  ldfr(NIT - NS + 1, afB, bfB); mma(afA, bfA); CORE_IL();
  if (NS == 8) {
    C::template wait_stages<5>(wid); BAR(); ldfr(NIT - 6, afA, bfA); mma(afB, bfB); CORE_IL();
    C::template wait_stages<4>(wid); BAR(); ldfr(NIT - 5, afB, bfB); mma(afA, bfA); CORE_IL();
    C::template wait_stages<3>(wid); BAR(); ldfr(NIT - 4, afA, bfA); mma(afB, bfB); CORE_IL();
    C::template wait_stages<2>(wid); BAR(); ldfr(NIT - 3, afB, bfB); mma(afA, bfA); CORE_IL();
  }
  C::template wait_stages<1>(wid); BAR(); ldfr(NIT - 2, afA, bfA); mma(afB, bfB); CORE_IL();
  C::template wait_stages<0>(wid); BAR(); ldfr(NIT - 1, afB, bfB); mma(afA, bfA); CORE_IL();
  mma(afB, bfB);
  STAMPK(KID, 2);
}

#define SCHED_IL() do { _Pragma("unroll") for (int _i = 0; _i < 9; ++_i) { __builtin_amdgcn_sched_group_barrier(0x100, 2, 0); __builtin_amdgcn_sched_group_barrier(0x8, 1, 0); } __builtin_amdgcn_sched_group_barrier(0x8, 32, 0); } while (0)

__device__ __forceinline__ bf16x8 tr_frag(const char* p) {
  typedef LDS_AS s16x4* lp;
  s16x4 lo = __builtin_amdgcn_ds_read_tr16_b64_v4i16((lp)(p));
  s16x4 hi = __builtin_amdgcn_ds_read_tr16_b64_v4i16((lp)(p + 4096));
  return __builtin_shufflevector(lo, hi, 0, 1, 2, 3, 4, 5, 6, 7);
}

template <int TYPE>
__device__ __forceinline__ void l1_wg(char* smem, const u16* __restrict__ xn, const u16* __restrict__ w1, const u16* __restrict__ wqkv,
                                      const u16* __restrict__ wf, u16* __restrict__ hbuf, u16* __restrict__ qb, float* __restrict__ kvslab,
                                      int rt, int ct) {
  constexpr int NCH = TYPE ? 38 : 54;
  constexpr int NTW = TYPE ? 5 : 7;
  const int tid = threadIdx.x, lane = tid & 63;
  const int wid = __builtin_amdgcn_readfirstlane(tid >> 6);
  const int wr = wid >> 2, wc = wid & 3, fr = lane & 15, fq = lane >> 4;
  const u16* ag = xn + (size_t)(rt * 128 + wid * 16 + (lane >> 2)) * D + (((lane & 3) ^ ((-(lane >> 4)) & 3)) * 8);
  f32x4 acc[4][NTW];
#pragma unroll
  for (int m = 0; m < 4; ++m)
#pragma unroll
    for (int j = 0; j < NTW; ++j) acc[m][j] = f32x4{0.f, 0.f, 0.f, 0.f};
  auto bsrc = [&](int k, int c, const u16*& p, int& st, int phase) {
    if (TYPE == 0) {
      int gc = ct * 432 + c * 8;
      if (gc < F) { p = w1 + (size_t)k * F + gc; st = 32 * F; }
      else { p = wf + (size_t)k * (2 * D) + (gc - F); st = 32 * 2 * D; }
    } else {
      if (c > 35) c = 35;
      int part = c >= 18 ? 1 : 0, cc = c - 18 * part;
      if (part == 0) { p = wf + (size_t)k * (2 * D) + D + ct * 144 + cc * 8; st = 32 * 2 * D; }
      else { p = wqkv + (size_t)k * (3 * D) + 2 * D + ct * 144 + cc * 8; st = 32 * 3 * D; }
    }
  };
  gemm_core<4, NTW, NCH, 4, 1>(18, smem, ag, bsrc, acc, wr * 64, NTW * wc, 1 << 30, nullptr);
  if (TYPE == 0) {
#pragma unroll
    for (int m = 0; m < 4; ++m)
#pragma unroll
      for (int j = 0; j < NTW; ++j) {
        int T = NTW * wc + j;
        if (T < 27) {
          int row = rt * 128 + wr * 64 + m * 16 + fr;
          int col0 = ct * 432 + T * 16 + fq * 4;
          f32x4 v = acc[m][j];
          if (col0 < F) {
            float o[4];
#pragma unroll
            for (int e = 0; e < 4; ++e) o[e] = v[e] * __builtin_amdgcn_rcpf(1.f + __builtin_amdgcn_exp2f(v[e] * -1.44269504f));
            typedef __attribute__((ext_vector_type(2))) unsigned u32x2;
            u32x2 pk = {pack2(o[0], o[1]), pack2(o[2], o[3])};
            __builtin_nontemporal_store(pk, (u32x2*)(hbuf + (size_t)row * F + col0));
          } else {
            uint2 pk = make_uint2(pack2(v[0], v[1]), pack2(v[2], v[3]));
            *(uint2*)(qb + (size_t)row * D + (col0 - F)) = pk;
          }
        }
      }
  } else {
    constexpr int PK_PITCH = 608, VT_PITCH = 352;
    char* Pk = smem;
    char* Vt = smem + 128 * PK_PITCH;
    __syncthreads();
#pragma unroll
    for (int m = 0; m < 4; ++m)
#pragma unroll
      for (int j = 0; j < NTW; ++j) {
        int T = NTW * wc + j;
        if (T < 18) {
          int row = wr * 64 + m * 16 + fr;
#pragma unroll
          for (int e = 0; e < 4; ++e) {
            int c = T * 16 + fq * 4 + e;
            int part = c >= 144 ? 1 : 0, cc = c - 144 * part, hd = cc / 18, d = cc - hd * 18;
            u16 val = f2bf(acc[m][j][e]);
            if (part == 0) *(u16*)(Pk + row * PK_PITCH + (hd * 36 + d) * 2) = val;
            else *(u16*)(Vt + row * VT_PITCH + (hd * 20 + d) * 2) = val;
          }
        }
      }
    for (int i = tid; i < 128 * 8; i += 512) { int row = i >> 3, hd = i & 7; *(u16*)(Vt + row * VT_PITCH + (hd * 20 + 18) * 2) = 0x3f80; }
    __syncthreads();
    {
      {
        unsigned* pr0 = (unsigned*)(Pk + lane * PK_PITCH + wid * 72);
        unsigned* pr1 = (unsigned*)(Pk + (lane + 64) * PK_PITCH + wid * 72);
        float p0[18], p1[18];
#pragma unroll
        for (int i = 0; i < 9; ++i) {
          unsigned u = pr0[i]; p0[2 * i] = __uint_as_float(u << 16); p0[2 * i + 1] = __uint_as_float(u & 0xffff0000u);
          unsigned w = pr1[i]; p1[2 * i] = __uint_as_float(w << 16); p1[2 * i + 1] = __uint_as_float(w & 0xffff0000u);
        }
        auto finish = [&](float* p, unsigned* pr) {
          float mx = -1e30f, mn = 1e30f;
#pragma unroll
          for (int f = 0; f < 18; ++f) { mx = fmaxf(mx, p[f]); mn = fminf(mn, p[f]); }
          float e1[18], e2[18], s1 = 0.f, s2 = 0.f;
#pragma unroll
          for (int f = 0; f < 18; ++f) { e1[f] = __expf(p[f] - mx); s1 += e1[f]; e2[f] = __expf(mn - p[f]); s2 += e2[f]; }
          float r1 = 1.f / s1, r2 = 1.f / s2;
#pragma unroll
          for (int i = 0; i < 9; ++i) { pr[i] = pack2(e1[2 * i] * r1, e1[2 * i + 1] * r1); pr[9 + i] = pack2(e2[2 * i] * r2, e2[2 * i + 1] * r2); }
        };
        finish(p0, pr0);
        finish(p1, pr1);
      }
    }
    __syncthreads();
    {
      typedef LDS_AS s16x4* lp;
      f32x4 kvacc[3][2];
#pragma unroll
      for (int a = 0; a < 3; ++a) for (int b = 0; b < 2; ++b) kvacc[a][b] = f32x4{0.f, 0.f, 0.f, 0.f};
      const int q = fr >> 2, p = lane & 3;
#pragma unroll
      for (int ks = 0; ks < 4; ++ks) {
        bf16x8 pa[3], vb[2];
        int r0 = ks * 32 + 8 * fq + q;
#pragma unroll
        for (int mt = 0; mt < 3; ++mt) {
          const char* a0 = Pk + r0 * PK_PITCH + (wid * 36 + mt * 16 + 4 * p) * 2;
          s16x4 lo = __builtin_amdgcn_ds_read_tr16_b64_v4i16((lp)(a0));
          s16x4 hi = __builtin_amdgcn_ds_read_tr16_b64_v4i16((lp)(a0 + 4 * PK_PITCH));
          pa[mt] = __builtin_shufflevector(lo, hi, 0, 1, 2, 3, 4, 5, 6, 7);
        }
#pragma unroll
        for (int nt = 0; nt < 2; ++nt) {
          const char* b0 = Vt + r0 * VT_PITCH + (wid * 20 + nt * 16 + 4 * p) * 2;
          s16x4 lo = __builtin_amdgcn_ds_read_tr16_b64_v4i16((lp)(b0));
          s16x4 hi = __builtin_amdgcn_ds_read_tr16_b64_v4i16((lp)(b0 + 4 * VT_PITCH));
          vb[nt] = __builtin_shufflevector(lo, hi, 0, 1, 2, 3, 4, 5, 6, 7);
        }
#pragma unroll
        for (int mt = 0; mt < 3; ++mt)
#pragma unroll
          for (int nt = 0; nt < 2; ++nt) kvacc[mt][nt] = __builtin_amdgcn_mfma_f32_16x16x32_bf16(pa[mt], vb[nt], kvacc[mt][nt], 0, 0, 0);
      }
      float* dst = kvslab + ((size_t)rt * 32 + ct * 8 + wid) * 720;
#pragma unroll
      for (int mt = 0; mt < 3; ++mt)
#pragma unroll
        for (int nt = 0; nt < 2; ++nt)
#pragma unroll
          for (int e = 0; e < 4; ++e) {
            int f = mt * 16 + fq * 4 + e, d = nt * 16 + fr;
            if (f < 36 && d < 19) dst[f * 20 + d] = kvacc[mt][nt][e];
          }
    }
  }
}

__global__ __launch_bounds__(512, 2) void k_l1(const u16* __restrict__ xn, const u16* __restrict__ w1, const u16* __restrict__ wqkv,
                                               const u16* __restrict__ wf, u16* __restrict__ hbuf, u16* __restrict__ qb, float* __restrict__ kvslab) {
  __shared__ __attribute__((aligned(16))) char smem[163840];
  int bid = blockIdx.x, xcd = bid & 7, idx = bid >> 3;
  int rt = (xcd & 1) * 8 + (idx & 7);
  int cs = (xcd >> 1) * 4 + (idx >> 3);
  if (cs < 12) l1_wg<0>(smem, xn, w1, wqkv, wf, hbuf, qb, kvslab, rt, cs);
  else l1_wg<1>(smem, xn, w1, wqkv, wf, hbuf, qb, kvslab, rt, cs - 12);
  STAMPK(1, 3);
}

constexpr int G4_A = 16384, G4_B = 18432, G4_STAGE = G4_A + G4_B;
__global__ __launch_bounds__(512, 2) void k_g2(const u16* __restrict__ hbuf, const u16* __restrict__ w2, const u16* __restrict__ o, const u16* __restrict__ wo,
                                               float* __restrict__ slab) {
  __shared__ __attribute__((aligned(16))) char smem[4 * G4_STAGE];
  int bid = blockIdx.x, xcd = bid & 7, idx = bid >> 3;
  int ks = xcd >> 1, rt = (xcd & 1) * 8 + (idx & 7), ct = idx >> 3;
  const int tid = threadIdx.x, lane = tid & 63, fr = lane & 15, fq = lane >> 4;
  const int wid = __builtin_amdgcn_readfirstlane(tid >> 6), wm = wid & 1, g = (wid >> 1) & 1, wn = wid >> 2;
  f32x4 acc[4][5];
#pragma unroll
  for (int m = 0; m < 4; ++m)
#pragma unroll
    for (int j = 0; j < 5; ++j) acc[m][j] = f32x4{0.f, 0.f, 0.f, 0.f};
  const int nst = ks == 3 ? 21 : 20, nsw = ks == 3 ? 12 : 100;
  const u16* ap[2]; const u16* bp[3];
  STAMPK(2, 0);
  auto setp = [&](const u16* ab, int lda, const u16* bb) {
#pragma unroll
    for (int i = 0; i < 2; ++i) {
      int row = (wid * 2 + i) * 8 + (lane >> 3), lc = (lane & 7) ^ ((row >> 1) & 7);
      ap[i] = ab + (size_t)(rt * 128 + row) * lda + lc * 8;
    }
#pragma unroll
    for (int i = 0; i < 3; ++i) {
      int ci = (wid + 8 * i) * 64 + lane; if (ci > 1151) ci = 1151;
      int rp = ci / 18, c = ci - rp * 18, k = (rp & ~31) | swap23(rp & 31);
      bp[i] = bb + (size_t)k * D + ct * 144 + c * 8;
    }
  };
  setp(hbuf + ks * 1280, F, w2 + (size_t)ks * 1280 * D);
  auto issue = [&](int t) {
    if (t == nsw) setp(o, D, wo);
    char* sb = smem + (t & 3) * G4_STAGE;
#pragma unroll
    for (int i = 0; i < 2; ++i) { GLDS(ap[i], sb + (wid * 2 + i) * 1024); ap[i] += 64; }
#pragma unroll
    for (int i = 0; i < 3; ++i) { if (i < 2 || wid < 2) GLDS(bp[i], sb + G4_A + (wid + 8 * i) * 1024); bp[i] += (size_t)64 * D; }
  };
  const int a_base = (wm * 64 + fr) * 128 + (((g * 4 + fq) ^ ((fr >> 1) & 7)) * 16);
  const int b_base = G4_A + (g * 32 + 16 * (fq >> 1) + 4 * (fq & 1) + (fr >> 2)) * 288 + (10 * wn + ((lane & 3) >> 1)) * 16 + 8 * (lane & 1);
  bf16x8 afA[4], bfA[5], afB[4], bfB[5];
  auto ldfr = [&](int t, bf16x8 (&af)[4], bf16x8 (&bf)[5]) {
    typedef LDS_AS s16x4* lp;
    const char* nb = smem + (t & 3) * G4_STAGE;
#pragma unroll
    for (int j = 0; j < 5; ++j) {
      s16x4 lo = __builtin_amdgcn_ds_read_tr16_b64_v4i16((lp)(nb + b_base + j * 32));
      s16x4 hi = __builtin_amdgcn_ds_read_tr16_b64_v4i16((lp)(nb + b_base + j * 32 + 8 * 288));
      bf[j] = __builtin_shufflevector(lo, hi, 0, 1, 2, 3, 4, 5, 6, 7);
    }
#pragma unroll
    for (int m = 0; m < 4; ++m) af[m] = *(const bf16x8*)(nb + a_base + m * 2048);
  };
  auto mma = [&](bf16x8 (&af)[4], bf16x8 (&bf)[5]) {
#pragma unroll
    for (int j = 0; j < 5; ++j)
      if (j < 4 || wn == 0)
#pragma unroll
      for (int m = 0; m < 4; ++m)
        acc[m][j] = __builtin_amdgcn_mfma_f32_16x16x32_bf16(bf[j], af[m], acc[m][j], 0, 0, 0);
  };
  auto waitst = [&](bool one) {
    if (wid < 2) { if (one) wait_v<5>(); else wait_v<0>(); } else { if (one) wait_v<4>(); else wait_v<0>(); }
  };
#define G4_IL() do { _Pragma("unroll") for (int _i = 0; _i < 14; ++_i) { __builtin_amdgcn_sched_group_barrier(0x100, 1, 0); __builtin_amdgcn_sched_group_barrier(0x8, 1, 0); } __builtin_amdgcn_sched_group_barrier(0x8, 64, 0); } while (0)
  issue(0); issue(1); issue(2);
  if (wid < 2) wait_v<10>(); else wait_v<8>();
  BAR();
  STAMPK(2, 1);
  ldfr(0, afA, bfA);
  int t = 0;
#pragma unroll 1
  for (; t + 2 < nst; t += 2) {
    waitst(t + 3 <= nst); BAR();
    if (t + 3 < nst) issue(t + 3);
    ldfr(t + 1, afB, bfB); mma(afA, bfA); G4_IL();
    waitst(t + 4 <= nst); BAR();
    if (t + 4 < nst) issue(t + 4);
    ldfr(t + 2, afA, bfA); mma(afB, bfB); G4_IL();
  }
  if (t + 1 < nst) {
    wait_v<0>(); BAR();
    ldfr(t + 1, afB, bfB); mma(afA, bfA); G4_IL();
    mma(afB, bfB);
  } else mma(afA, bfA);
  STAMPK(2, 2);
  __syncthreads();
  f32x4* red = (f32x4*)smem;
  const int rs = (wn * 2 + wm) * 20;
  if (g == 1) {
#pragma unroll
    for (int m = 0; m < 4; ++m)
#pragma unroll
      for (int j = 0; j < 5; ++j) red[(rs + m * 5 + j) * 64 + lane] = acc[m][j];
  }
  __syncthreads();
  if (g == 0) {
    float* dst = slab + (size_t)ks * S * D;
#pragma unroll
    for (int m = 0; m < 4; ++m)
#pragma unroll
      for (int j = 0; j < 5; ++j)
        if (wn == 0 || j < 4) __builtin_nontemporal_store(acc[m][j] + red[(rs + m * 5 + j) * 64 + lane], (f32x4*)(dst + (size_t)(rt * 128 + wm * 64 + m * 16 + fr) * D + ct * 144 + (wn * 5 + j) * 16 + fq * 4));
  }
  STAMPK(2, 3);
}

__global__ __launch_bounds__(256) void k_l3(const float* __restrict__ slab, const u16* __restrict__ x, u16* __restrict__ out) {
  int i = (blockIdx.x * 256 + threadIdx.x) * 4;
  f32x4 v = *(const f32x4*)(slab + i);
#pragma unroll
  for (int k = 1; k < 4; ++k) v += *(const f32x4*)(slab + (size_t)k * S * D + i);
  uint2 xv = *(const uint2*)(x + i);
  float x0 = __uint_as_float(xv.x << 16), x1 = __uint_as_float(xv.x & 0xffff0000u), x2 = __uint_as_float(xv.y << 16), x3 = __uint_as_float(xv.y & 0xffff0000u);
  *(uint2*)(out + i) = make_uint2(pack2(v[0] + x0, v[1] + x1), pack2(v[2] + x2, v[3] + x3));
}

__global__ __launch_bounds__(256) void k_qo3(const u16* __restrict__ pq, const float* __restrict__ kvslab, u16* __restrict__ o) {
  __shared__ __attribute__((aligned(16))) u16 kvT[32 * 72];
  const int h = blockIdx.x >> 3, rg = blockIdx.x & 7, tid = threadIdx.x, lane = tid & 63, wid = tid >> 6;
  const int fr = lane & 15, fq = lane >> 4;
  const int base = fq * 5 - (fq == 3 ? 1 : 0), cnt = fq < 2 ? 5 : 4;
  float p[4][5];
#pragma unroll
  for (int mt = 0; mt < 4; ++mt) {
    const u16* pr = pq + (size_t)(rg * 256 + wid * 64 + mt * 16 + fr) * D + h * DH + base;
#pragma unroll
    for (int j = 0; j < 5; ++j) p[mt][j] = bf2f(pr[j < cnt ? j : 0]);
  }
  {
    float v[3][16];
#pragma unroll
    for (int i = 0; i < 3; ++i) {
      int e = tid + 256 * i; if (e > 683) e = 683;
      int f = e / 19, n = e - f * 19;
#pragma unroll
      for (int r = 0; r < 16; ++r) v[i][r] = kvslab[((size_t)r * 32 + h) * 720 + f * 20 + n];
    }
    for (int i = tid; i < 32 * 36; i += 256) ((unsigned*)kvT)[i] = 0u;
    __syncthreads();
#pragma unroll
    for (int i = 0; i < 3; ++i) {
      int e = tid + 256 * i;
      float t = 0.f;
#pragma unroll
      for (int r = 0; r < 16; ++r) t += v[i][r];
      if (e < 684) {
        int f = e / 19, n = e - f * 19;
        int s = f >= 18 ? 1 : 0, d = f - 18 * s;
        int q = d < 10 ? d / 5 : 2 + (d - 10) / 4;
        int j = d - (q * 5 - (q == 3 ? 1 : 0));
        kvT[n * 72 + s * 32 + q * 8 + j] = f2bf(t);
      }
    }
  }
  __syncthreads();
  bf16x8 bfr[2][2];
#pragma unroll
  for (int s = 0; s < 2; ++s)
#pragma unroll
    for (int j = 0; j < 2; ++j) bfr[s][j] = *(const bf16x8*)(kvT + (j * 16 + fr) * 72 + s * 32 + fq * 8);
#pragma unroll
  for (int mt = 0; mt < 4; ++mt) {
    float mx = p[mt][0], mn = p[mt][0];
#pragma unroll
    for (int j = 1; j < 5; ++j) { mx = fmaxf(mx, p[mt][j]); mn = fminf(mn, p[mt][j]); }
    mx = fmaxf(mx, __shfl_xor(mx, 16)); mx = fmaxf(mx, __shfl_xor(mx, 32));
    mn = fminf(mn, __shfl_xor(mn, 16)); mn = fminf(mn, __shfl_xor(mn, 32));
    float e1[5], e2[5], s1 = 0.f, s2 = 0.f;
#pragma unroll
    for (int j = 0; j < 5; ++j) {
      e1[j] = j < cnt ? __expf(p[mt][j] - mx) : 0.f; e2[j] = j < cnt ? __expf(mn - p[mt][j]) : 0.f;
      s1 += e1[j]; s2 += e2[j];
    }
    s1 += __shfl_xor(s1, 16); s1 += __shfl_xor(s1, 32);
    s2 += __shfl_xor(s2, 16); s2 += __shfl_xor(s2, 32);
    float r1 = 1.f / s1, r2 = 1.f / s2;
    typedef __attribute__((ext_vector_type(4))) unsigned u32x4;
    u32x4 a0 = {pack2(e1[0] * r1, e1[1] * r1), pack2(e1[2] * r1, e1[3] * r1), pack2(e1[4] * r1, 0.f), 0u};
    u32x4 a1 = {pack2(e2[0] * r2, e2[1] * r2), pack2(e2[2] * r2, e2[3] * r2), pack2(e2[4] * r2, 0.f), 0u};
    bf16x8 af0 = __builtin_bit_cast(bf16x8, a0), af1 = __builtin_bit_cast(bf16x8, a1);
    f32x4 acc[2];
#pragma unroll
    for (int j = 0; j < 2; ++j) {
      acc[j] = f32x4{0.f, 0.f, 0.f, 0.f};
      acc[j] = __builtin_amdgcn_mfma_f32_16x16x32_bf16(bfr[0][j], af0, acc[j], 0, 0, 0);
      acc[j] = __builtin_amdgcn_mfma_f32_16x16x32_bf16(bfr[1][j], af1, acc[j], 0, 0, 0);
    }
    float den = rbf(__shfl(acc[1][2], fr)) + 1e-6f;
    float rd = 1.f / den;
    unsigned* dst = (unsigned*)(o + (size_t)(rg * 256 + wid * 64 + mt * 16 + fr) * D + h * DH);
    dst[2 * fq] = pack2(rbf(acc[0][0]) * rd, rbf(acc[0][1]) * rd);
    dst[2 * fq + 1] = pack2(rbf(acc[0][2]) * rd, rbf(acc[0][3]) * rd);
    if (fq == 0) dst[8] = pack2(rbf(acc[1][0]) * rd, rbf(acc[1][1]) * rd);
  }
}

extern "C" void kernel_launch(void* const* d_in, const int* in_sizes, int n_in, void* d_out, int out_size, void* d_ws, size_t ws_size, hipStream_t stream) {
  const u16* x = (const u16*)d_in[0]; const u16* wqkv = (const u16*)d_in[1]; const u16* wphi = (const u16*)d_in[2]; const u16* wo = (const u16*)d_in[3];
  const u16* w1 = (const u16*)d_in[5]; const u16* w2 = (const u16*)d_in[6];
  u16* out = (u16*)d_out;
  char* ws = (char*)d_ws;
  u16* xn = (u16*)ws; ws += (size_t)S * D * 2;
  u16* qb = (u16*)ws; ws += (size_t)S * D * 2;
  u16* hbuf = (u16*)ws; ws += (size_t)S * F * 2;
  float* kvslab = (float*)ws; ws += 16 * 32 * 720 * 4;
  float* kv = (float*)ws; ws += 32 * 720 * 4;
  u16* o = (u16*)ws; ws += (size_t)S * D * 2;
  float* slab = (float*)ws; ws += (size_t)4 * S * D * 4;
  u16* wf = (u16*)ws; ws += (size_t)D * 2 * D * 2;
  k_ln<<<S / 4 + 144 + 128, 256, 0, stream>>>(x, xn, wqkv, wphi, wf, w1, w2);
  k_l1<<<256, 512, 0, stream>>>(xn, w1, wqkv, wf, hbuf, qb, kvslab);
  k_qo3<<<256, 256, 0, stream>>>(qb, kvslab, o);
  k_g2<<<256, 512, 0, stream>>>(hbuf, w2, o, wo, slab);
  k_l3<<<S * D / 1024, 256, 0, stream>>>(slab, x, out);
}
```

```cpp
#include <hip/hip_runtime.h>
#include <hip/hip_bf16.h>
#include <stdint.h>

typedef __attribute__((ext_vector_type(8))) short bf16x8;
typedef __attribute__((ext_vector_type(4))) short s16x4;
typedef __attribute__((ext_vector_type(4))) float f32x4;
typedef unsigned short u16;
#define LDS_AS __attribute__((address_space(3)))

__device__ __forceinline__ float bf2f(u16 v) { return __uint_as_float(((unsigned)v) << 16); }
__device__ __forceinline__ u16 f2bf(float f) {
  unsigned u = __float_as_uint(f);
  u += 0x7fffu + ((u >> 16) & 1u);
  return (u16)(u >> 16);
}
__device__ __forceinline__ unsigned pack2(float lo, float hi) { unsigned r; asm("v_cvt_pk_bf16_f32 %0, %1, %2" : "=v"(r) : "v"(lo), "v"(hi)); return r; }
__device__ __forceinline__ float rbf(float f) { return __uint_as_float(pack2(f, f) << 16); }

constexpr int S = 2048, D = 576, H = 32, DH = 18, F = 4608;

#define STAMPK(k, i)
__global__ __launch_bounds__(256) void k_ln(const u16* __restrict__ x, u16* __restrict__ xn, const u16* __restrict__ wqkv, const u16* __restrict__ wphi, u16* __restrict__ wf,
                                            const u16* __restrict__ w1, const u16* __restrict__ w2, unsigned* __restrict__ dummy) {
  if (blockIdx.x >= S / 4 + 144) {
    int t = (blockIdx.x - S / 4 - 144) * 256 + threadIdx.x;
    const uint4* p1 = (const uint4*)w1; const uint4* p2 = (const uint4*)w2;
    unsigned a = 0;
#pragma unroll
    for (int i = 0; i < 10; ++i) { uint4 v = p1[t + i * 32768 < 331776 ? t + i * 32768 : 0]; uint4 u = p2[t + i * 32768 < 331776 ? t + i * 32768 : 0]; a ^= v.x ^ v.w ^ u.x ^ u.w; }
    if (a == 0x9e3779b9u) dummy[0] = a;
    return;
  }
  if (blockIdx.x >= S / 4) {
    int u = __builtin_amdgcn_readfirstlane((blockIdx.x - S / 4) * 4 + (threadIdx.x >> 6));
    int wh = u & 63, k = (u >> 6) * 64 + (threadIdx.x & 63), h = wh & 31;
    const u16* src = wqkv + (size_t)k * (3 * D) + wh * 18;
    const unsigned* W = (const unsigned*)(wphi + h * 324);
    float a[18];
#pragma unroll
    for (int i = 0; i < 9; ++i) { unsigned v = *(const unsigned*)(src + 2 * i); a[2 * i] = __uint_as_float(v << 16); a[2 * i + 1] = __uint_as_float(v & 0xffff0000u); }
    u16* dst = wf + (size_t)k * (2 * D) + wh * 18;
#pragma unroll
    for (int f = 0; f < 18; f += 2) {
      float s0 = 0.f, s1 = 0.f;
#pragma unroll
      for (int d = 0; d < 18; ++d) { unsigned w = W[d * 9 + (f >> 1)]; s0 = fmaf(a[d], __uint_as_float(w << 16), s0); s1 = fmaf(a[d], __uint_as_float(w & 0xffff0000u), s1); }
      *(unsigned*)(dst + f) = pack2(s0, s1);
    }
    return;
  }
  int row = blockIdx.x * 4 + (threadIdx.x >> 6), lane = threadIdx.x & 63;
  const u16* xr = x + (size_t)row * D;
  float v[9]; float s = 0.f;
  for (int i = 0; i < 9; ++i) { v[i] = bf2f(xr[lane + 64 * i]); s += v[i]; }
  for (int o = 32; o; o >>= 1) s += __shfl_xor(s, o);
  float m = s * (1.f / D);
  float q = 0.f;
  for (int i = 0; i < 9; ++i) { float d = v[i] - m; q += d * d; }
  for (int o = 32; o; o >>= 1) q += __shfl_xor(q, o);
  float r = rsqrtf(q * (1.f / D) + 1e-5f);
  for (int i = 0; i < 9; ++i) xn[(size_t)row * D + lane + 64 * i] = f2bf((v[i] - m) * r);
}

__device__ __forceinline__ void glds16_asm(const void* gsrc, unsigned lds_dst) {
  unsigned keep;
  asm volatile("s_mov_b32 %0, m0\n\ts_mov_b32 m0, %2\n\ts_nop 0\n\tglobal_load_lds_dwordx4 %1, off\n\ts_mov_b32 m0, %0" : "=&s"(keep) : "v"(gsrc), "s"(lds_dst) : "memory");
}
#define GLDS(g, l) glds16_asm((const void*)(g), (unsigned)(size_t)(l))
#define WAIT_V(n) asm volatile("s_waitcnt vmcnt(" #n ")" ::: "memory")
#define WAIT_L0() asm volatile("s_waitcnt lgkmcnt(0)" ::: "memory")
#define BAR() __builtin_amdgcn_s_barrier()

__device__ __forceinline__ int swap23(int r) { return (r & ~0xC) | ((r & 4) << 1) | ((r & 8) >> 1); }

template <int N> __device__ __forceinline__ void wait_v() { asm volatile("s_waitcnt vmcnt(%0)" :: "n"(N) : "memory"); }
template <int MT, int NTW, int NCH, int NS>
struct Core {
  static constexpr int PITCH = NCH * 16;
  static constexpr int NB = (32 * NCH + 63) / 64;
  static constexpr int STAGE = 8192 + NB * 1024;
  static constexpr int NLO = 1 + NB / 8, NHI = 1 + (NB + 7) / 8, XW = NB % 8;
  template <int K> static __device__ __forceinline__ void wait_stages(int wid) {
    if (XW == 0) wait_v<K * NLO>();
    else { if (wid < XW) wait_v<K * NHI>(); else wait_v<K * NLO>(); }
  }
};

template <int MT, int NTW, int NCH, int NS, int KID, class BSRC>
__device__ __forceinline__ void gemm_core(int NIT, char* smem, const u16* ag0, BSRC&& bsrc, f32x4 (&acc)[MT][NTW], int a_row0  , int tile0  ,
                                          int nsw, const u16* ag1) {
  using C = Core<MT, NTW, NCH, NS>;
  const int tid = threadIdx.x, lane = tid & 63;
  const int wid = __builtin_amdgcn_readfirstlane(tid >> 6);
  const int fr = lane & 15, fq = lane >> 4;
  const u16* ag = ag0;
  const u16* bg[4]; int bst[4];
#pragma unroll
  for (int i = 0; i < 4; ++i) {
    int ci = (wid + 8 * i) * 64 + lane;
    int rho = ci / NCH, c = ci - rho * NCH;
    if (rho > 31) { rho = 31; }
    bsrc(swap23(rho), c, bg[i], bst[i], 0);
  }
  int nissued = 0;
  auto issue = [&](int stg) {
    if (nissued == nsw) {
      ag = ag1;
#pragma unroll
      for (int i = 0; i < 4; ++i) {
        int ci = (wid + 8 * i) * 64 + lane;
        int rho = ci / NCH, c = ci - rho * NCH;
        if (rho > 31) { rho = 31; }
        bsrc(swap23(rho), c, bg[i], bst[i], 1);
      }
    }
    ++nissued;
    char* sb = smem + (stg % NS) * C::STAGE;
    GLDS(ag, sb + wid * 1024); ag += 32;
#pragma unroll
    for (int i = 0; i < 4; ++i) {
      if (8 * i < C::NB && (8 * i + 8 <= C::NB || wid < C::NB - 8 * i)) GLDS(bg[i], sb + 8192 + (wid + 8 * i) * 1024);
      bg[i] += bst[i];
    }
  };
  const int a_base = (a_row0 + fr) * 64 + ((fq ^ ((-(fr >> 2)) & 3)) * 16);
  const int b_base = 8192 + (16 * (fq >> 1) + 4 * (fq & 1) + (fr >> 2)) * C::PITCH + (2 * tile0 + ((lane & 3) >> 1)) * 16 + 8 * (lane & 1);
  bf16x8 afA[MT], bfA[NTW], afB[MT], bfB[NTW];
  auto ldfr = [&](int stg, bf16x8 (&af)[MT], bf16x8 (&bf)[NTW]) {
    typedef LDS_AS s16x4* lp;
    const char* nb = smem + (stg % NS) * C::STAGE;
#pragma unroll
    for (int j = 0; j < NTW; ++j) {
      s16x4 lo = __builtin_amdgcn_ds_read_tr16_b64_v4i16((lp)(nb + b_base + j * 32));
      s16x4 hi = __builtin_amdgcn_ds_read_tr16_b64_v4i16((lp)(nb + b_base + j * 32 + 8 * C::PITCH));
      bf[j] = __builtin_shufflevector(lo, hi, 0, 1, 2, 3, 4, 5, 6, 7);
    }
#pragma unroll
    for (int m = 0; m < MT; ++m) af[m] = *(const bf16x8*)(nb + a_base + m * 1024);
  };
  auto mma = [&](bf16x8 (&af)[MT], bf16x8 (&bf)[NTW]) {
#pragma unroll
    for (int j = 0; j < NTW; ++j)
#pragma unroll
      for (int m = 0; m < MT; ++m)
        acc[m][j] = __builtin_amdgcn_mfma_f32_16x16x32_bf16(bf[j], af[m], acc[m][j], 0, 0, 0);
  };
  constexpr int NRD = 2 * NTW + MT, NMM = MT * NTW;
  constexpr int MPR = NMM / NRD > 0 ? NMM / NRD : 1;
#define CORE_IL() do { _Pragma("unroll") for (int _i = 0; _i < NRD; ++_i) { __builtin_amdgcn_sched_group_barrier(0x100, 1, 0); __builtin_amdgcn_sched_group_barrier(0x8, MPR, 0); } __builtin_amdgcn_sched_group_barrier(0x8, 64, 0); } while (0)
  STAMPK(KID, 0);
#pragma unroll
  for (int s = 0; s < NS - 1; ++s) issue(s);
  C::template wait_stages<NS - 2>(wid);
  BAR();
  STAMPK(KID, 1);
  ldfr(0, afA, bfA);
#pragma unroll 1
  for (int t = 0; t + NS < NIT; t += 2) {
    C::template wait_stages<NS - 3>(wid); BAR();
    issue(t + NS - 1);
    ldfr(t + 1, afB, bfB); mma(afA, bfA); CORE_IL();
    C::template wait_stages<NS - 3>(wid); BAR();
    issue(t + NS);
    ldfr(t + 2, afA, bfA); mma(afB, bfB); CORE_IL();
  }
  static_assert(NS == 4 || NS == 8, "NS");
  C::template wait_stages<NS - 3>(wid); BAR();
  issue(NIT - 1);
  ldfr(NIT - NS + 1, afB, bfB); mma(afA, bfA); CORE_IL();
  if (NS == 8) {
    C::template wait_stages<5>(wid); BAR(); ldfr(NIT - 6, afA, bfA); mma(afB, bfB); CORE_IL();
    C::template wait_stages<4>(wid); BAR(); ldfr(NIT - 5, afB, bfB); mma(afA, bfA); CORE_IL();
    C::template wait_stages<3>(wid); BAR(); ldfr(NIT - 4, afA, bfA); mma(afB, bfB); CORE_IL();
    C::template wait_stages<2>(wid); BAR(); ldfr(NIT - 3, afB, bfB); mma(afA, bfA); CORE_IL();
  }
  C::template wait_stages<1>(wid); BAR(); ldfr(NIT - 2, afA, bfA); mma(afB, bfB); CORE_IL();
  C::template wait_stages<0>(wid); BAR(); ldfr(NIT - 1, afB, bfB); mma(afA, bfA); CORE_IL();
  mma(afB, bfB);
  STAMPK(KID, 2);
}

#define SCHED_IL() do { _Pragma("unroll") for (int _i = 0; _i < 9; ++_i) { __builtin_amdgcn_sched_group_barrier(0x100, 2, 0); __builtin_amdgcn_sched_group_barrier(0x8, 1, 0); } __builtin_amdgcn_sched_group_barrier(0x8, 32, 0); } while (0)

__device__ __forceinline__ bf16x8 tr_frag(const char* p) {
  typedef LDS_AS s16x4* lp;
  s16x4 lo = __builtin_amdgcn_ds_read_tr16_b64_v4i16((lp)(p));
  s16x4 hi = __builtin_amdgcn_ds_read_tr16_b64_v4i16((lp)(p + 4096));
  return __builtin_shufflevector(lo, hi, 0, 1, 2, 3, 4, 5, 6, 7);
}

template <int TYPE>
__device__ __forceinline__ void l1_wg(char* smem, const u16* __restrict__ xn, const u16* __restrict__ w1, const u16* __restrict__ wqkv,
                                      const u16* __restrict__ wf, u16* __restrict__ hbuf, u16* __restrict__ qb, float* __restrict__ kvslab,
                                      int rt, int ct) {
  constexpr int NCH = TYPE ? 38 : 54;
  constexpr int NTW = TYPE ? 5 : 7;
  const int tid = threadIdx.x, lane = tid & 63;
  const int wid = __builtin_amdgcn_readfirstlane(tid >> 6);
  const int wr = wid >> 2, wc = wid & 3, fr = lane & 15, fq = lane >> 4;
  const u16* ag = xn + (size_t)(rt * 128 + wid * 16 + (lane >> 2)) * D + (((lane & 3) ^ ((-(lane >> 4)) & 3)) * 8);
  f32x4 acc[4][NTW];
#pragma unroll
  for (int m = 0; m < 4; ++m)
#pragma unroll
    for (int j = 0; j < NTW; ++j) acc[m][j] = f32x4{0.f, 0.f, 0.f, 0.f};
  auto bsrc = [&](int k, int c, const u16*& p, int& st, int phase) {
    if (TYPE == 0) {
      int gc = ct * 432 + c * 8;
      if (gc < F) { p = w1 + (size_t)k * F + gc; st = 32 * F; }
      else { p = wf + (size_t)k * (2 * D) + (gc - F); st = 32 * 2 * D; }
    } else {
      if (c > 35) c = 35;
      int part = c >= 18 ? 1 : 0, cc = c - 18 * part;
      if (part == 0) { p = wf + (size_t)k * (2 * D) + D + ct * 144 + cc * 8; st = 32 * 2 * D; }
      else { p = wqkv + (size_t)k * (3 * D) + 2 * D + ct * 144 + cc * 8; st = 32 * 3 * D; }
    }
  };
  gemm_core<4, NTW, NCH, 4, 1>(18, smem, ag, bsrc, acc, wr * 64, NTW * wc, 1 << 30, nullptr);
  if (TYPE == 0) {
#pragma unroll
    for (int m = 0; m < 4; ++m)
#pragma unroll
      for (int j = 0; j < NTW; ++j) {
        int T = NTW * wc + j;
        if (T < 27) {
          int row = rt * 128 + wr * 64 + m * 16 + fr;
          int col0 = ct * 432 + T * 16 + fq * 4;
          f32x4 v = acc[m][j];
          if (col0 < F) {
            float o[4];
#pragma unroll
            for (int e = 0; e < 4; ++e) o[e] = v[e] * __builtin_amdgcn_rcpf(1.f + __builtin_amdgcn_exp2f(v[e] * -1.44269504f));
            uint2 pk = make_uint2(pack2(o[0], o[1]), pack2(o[2], o[3]));
            *(uint2*)(hbuf + (size_t)row * F + col0) = pk;
          } else {
            uint2 pk = make_uint2(pack2(v[0], v[1]), pack2(v[2], v[3]));
            *(uint2*)(qb + (size_t)row * D + (col0 - F)) = pk;
          }
        }
      }
  } else {
    constexpr int PK_PITCH = 608, VT_PITCH = 352;
    char* Pk = smem;
    char* Vt = smem + 128 * PK_PITCH;
    __syncthreads();
#pragma unroll
    for (int m = 0; m < 4; ++m)
#pragma unroll
      for (int j = 0; j < NTW; ++j) {
        int T = NTW * wc + j;
        if (T < 18) {
          int row = wr * 64 + m * 16 + fr;
#pragma unroll
          for (int e = 0; e < 4; ++e) {
            int c = T * 16 + fq * 4 + e;
            int part = c >= 144 ? 1 : 0, cc = c - 144 * part, hd = cc / 18, d = cc - hd * 18;
            u16 val = f2bf(acc[m][j][e]);
            if (part == 0) *(u16*)(Pk + row * PK_PITCH + (hd * 36 + d) * 2) = val;
            else *(u16*)(Vt + row * VT_PITCH + (hd * 20 + d) * 2) = val;
          }
        }
      }
    for (int i = tid; i < 128 * 8; i += 512) { int row = i >> 3, hd = i & 7; *(u16*)(Vt + row * VT_PITCH + (hd * 20 + 18) * 2) = 0x3f80; }
    __syncthreads();
    {
      {
        unsigned* pr0 = (unsigned*)(Pk + lane * PK_PITCH + wid * 72);
        unsigned* pr1 = (unsigned*)(Pk + (lane + 64) * PK_PITCH + wid * 72);
        float p0[18], p1[18];
#pragma unroll
        for (int i = 0; i < 9; ++i) {
          unsigned u = pr0[i]; p0[2 * i] = __uint_as_float(u << 16); p0[2 * i + 1] = __uint_as_float(u & 0xffff0000u);
          unsigned w = pr1[i]; p1[2 * i] = __uint_as_float(w << 16); p1[2 * i + 1] = __uint_as_float(w & 0xffff0000u);
        }
        auto finish = [&](float* p, unsigned* pr) {
          float mx = -1e30f, mn = 1e30f;
#pragma unroll
          for (int f = 0; f < 18; ++f) { mx = fmaxf(mx, p[f]); mn = fminf(mn, p[f]); }
          float e1[18], e2[18], s1 = 0.f, s2 = 0.f;
#pragma unroll
          for (int f = 0; f < 18; ++f) { e1[f] = __expf(p[f] - mx); s1 += e1[f]; e2[f] = __expf(mn - p[f]); s2 += e2[f]; }
          float r1 = 1.f / s1, r2 = 1.f / s2;
#pragma unroll
          for (int i = 0; i < 9; ++i) { pr[i] = pack2(e1[2 * i] * r1, e1[2 * i + 1] * r1); pr[9 + i] = pack2(e2[2 * i] * r2, e2[2 * i + 1] * r2); }
        };
        finish(p0, pr0);
        finish(p1, pr1);
      }
    }
    __syncthreads();
    {
      typedef LDS_AS s16x4* lp;
      f32x4 kvacc[3][2];
#pragma unroll
      for (int a = 0; a < 3; ++a) for (int b = 0; b < 2; ++b) kvacc[a][b] = f32x4{0.f, 0.f, 0.f, 0.f};
      const int q = fr >> 2, p = lane & 3;
#pragma unroll
      for (int ks = 0; ks < 4; ++ks) {
        bf16x8 pa[3], vb[2];
        int r0 = ks * 32 + 8 * fq + q;
#pragma unroll
        for (int mt = 0; mt < 3; ++mt) {
          const char* a0 = Pk + r0 * PK_PITCH + (wid * 36 + mt * 16 + 4 * p) * 2;
          s16x4 lo = __builtin_amdgcn_ds_read_tr16_b64_v4i16((lp)(a0));
          s16x4 hi = __builtin_amdgcn_ds_read_tr16_b64_v4i16((lp)(a0 + 4 * PK_PITCH));
          pa[mt] = __builtin_shufflevector(lo, hi, 0, 1, 2, 3, 4, 5, 6, 7);
        }
#pragma unroll
        for (int nt = 0; nt < 2; ++nt) {
          const char* b0 = Vt + r0 * VT_PITCH + (wid * 20 + nt * 16 + 4 * p) * 2;
          s16x4 lo = __builtin_amdgcn_ds_read_tr16_b64_v4i16((lp)(b0));
          s16x4 hi = __builtin_amdgcn_ds_read_tr16_b64_v4i16((lp)(b0 + 4 * VT_PITCH));
          vb[nt] = __builtin_shufflevector(lo, hi, 0, 1, 2, 3, 4, 5, 6, 7);
        }
#pragma unroll
        for (int mt = 0; mt < 3; ++mt)
#pragma unroll
          for (int nt = 0; nt < 2; ++nt) kvacc[mt][nt] = __builtin_amdgcn_mfma_f32_16x16x32_bf16(pa[mt], vb[nt], kvacc[mt][nt], 0, 0, 0);
      }
      float* dst = kvslab + ((size_t)rt * 32 + ct * 8 + wid) * 720;
#pragma unroll
      for (int mt = 0; mt < 3; ++mt)
#pragma unroll
        for (int nt = 0; nt < 2; ++nt)
#pragma unroll
          for (int e = 0; e < 4; ++e) {
            int f = mt * 16 + fq * 4 + e, d = nt * 16 + fr;
            if (f < 36 && d < 19) dst[f * 20 + d] = kvacc[mt][nt][e];
          }
    }
  }
}

__global__ __launch_bounds__(512, 2) void k_l1(const u16* __restrict__ xn, const u16* __restrict__ w1, const u16* __restrict__ wqkv,
                                               const u16* __restrict__ wf, u16* __restrict__ hbuf, u16* __restrict__ qb, float* __restrict__ kvslab) {
  __shared__ __attribute__((aligned(16))) char smem[163840];
  int bid = blockIdx.x, xcd = bid & 7, idx = bid >> 3;
  int rt = (xcd & 1) * 8 + (idx & 7);
  int cs = (xcd >> 1) * 4 + (idx >> 3);
  if (cs < 12) l1_wg<0>(smem, xn, w1, wqkv, wf, hbuf, qb, kvslab, rt, cs);
  else l1_wg<1>(smem, xn, w1, wqkv, wf, hbuf, qb, kvslab, rt, cs - 12);
  STAMPK(1, 3);
}

constexpr int G4_A = 16384, G4_B = 18432, G4_STAGE = G4_A + G4_B;
template <int NT>
__device__ __forceinline__ void g2_body(char* smem, const u16* __restrict__ hbuf, const u16* __restrict__ w2, const u16* __restrict__ o, const u16* __restrict__ wo,
                                        float* __restrict__ slab, int ks, int rt, int ct) {
  const int tid = threadIdx.x, lane = tid & 63, fr = lane & 15, fq = lane >> 4;
  const int wid = __builtin_amdgcn_readfirstlane(tid >> 6), wm = wid & 1, g = (wid >> 1) & 1;
  constexpr int wn = NT == 5 ? 0 : 1;
  f32x4 acc[4][NT];
#pragma unroll
  for (int m = 0; m < 4; ++m)
#pragma unroll
    for (int j = 0; j < NT; ++j) acc[m][j] = f32x4{0.f, 0.f, 0.f, 0.f};
  const int nst = ks == 3 ? 21 : 20, nsw = ks == 3 ? 12 : 100;
  const u16* ap[2]; const u16* bp[3];
  STAMPK(2, 0);
  auto setp = [&](const u16* ab, int lda, const u16* bb) {
#pragma unroll
    for (int i = 0; i < 2; ++i) {
      int row = (wid * 2 + i) * 8 + (lane >> 3), lc = (lane & 7) ^ ((row >> 1) & 7);
      ap[i] = ab + (size_t)(rt * 128 + row) * lda + lc * 8;
    }
#pragma unroll
    for (int i = 0; i < 3; ++i) {
      int ci = (wid + 8 * i) * 64 + lane; if (ci > 1151) ci = 1151;
      int rp = ci / 18, c = ci - rp * 18, k = (rp & ~31) | swap23(rp & 31);
      bp[i] = bb + (size_t)k * D + ct * 144 + c * 8;
    }
  };
  setp(hbuf + ks * 1280, F, w2 + (size_t)ks * 1280 * D);
  auto issue = [&](int t) {
    if (t == nsw) setp(o, D, wo);
    char* sb = smem + (t & 3) * G4_STAGE;
#pragma unroll
    for (int i = 0; i < 2; ++i) { GLDS(ap[i], sb + (wid * 2 + i) * 1024); ap[i] += 64; }
#pragma unroll
    for (int i = 0; i < 3; ++i) { if (i < 2 || wid < 2) GLDS(bp[i], sb + G4_A + (wid + 8 * i) * 1024); bp[i] += (size_t)64 * D; }
  };
  const int a_base = (wm * 64 + fr) * 128 + (((g * 4 + fq) ^ ((fr >> 1) & 7)) * 16);
  const int b_base = G4_A + (g * 32 + 16 * (fq >> 1) + 4 * (fq & 1) + (fr >> 2)) * 288 + (10 * wn + ((lane & 3) >> 1)) * 16 + 8 * (lane & 1);
  bf16x8 afA[4], bfA[NT], afB[4], bfB[NT];
  auto ldfr = [&](int t, bf16x8 (&af)[4], bf16x8 (&bf)[NT]) {
    typedef LDS_AS s16x4* lp;
    const char* nb = smem + (t & 3) * G4_STAGE;
#pragma unroll
    for (int j = 0; j < NT; ++j) {
      s16x4 lo = __builtin_amdgcn_ds_read_tr16_b64_v4i16((lp)(nb + b_base + j * 32));
      s16x4 hi = __builtin_amdgcn_ds_read_tr16_b64_v4i16((lp)(nb + b_base + j * 32 + 8 * 288));
      bf[j] = __builtin_shufflevector(lo, hi, 0, 1, 2, 3, 4, 5, 6, 7);
    }
#pragma unroll
    for (int m = 0; m < 4; ++m) af[m] = *(const bf16x8*)(nb + a_base + m * 2048);
  };
  auto mma = [&](bf16x8 (&af)[4], bf16x8 (&bf)[NT]) {
#pragma unroll
    for (int j = 0; j < NT; ++j)
#pragma unroll
      for (int m = 0; m < 4; ++m)
        acc[m][j] = __builtin_amdgcn_mfma_f32_16x16x32_bf16(bf[j], af[m], acc[m][j], 0, 0, 0);
  };
  auto waitst = [&](bool one) {
    if (wid < 2) { if (one) wait_v<5>(); else wait_v<0>(); } else { if (one) wait_v<4>(); else wait_v<0>(); }
  };
#define G4_IL() do { _Pragma("unroll") for (int _i = 0; _i < 2 * NT + 4; ++_i) { __builtin_amdgcn_sched_group_barrier(0x100, 1, 0); __builtin_amdgcn_sched_group_barrier(0x8, 1, 0); } __builtin_amdgcn_sched_group_barrier(0x8, 64, 0); } while (0)
  issue(0); issue(1); issue(2);
  if (wid < 2) wait_v<10>(); else wait_v<8>();
  BAR();
  STAMPK(2, 1);
  ldfr(0, afA, bfA);
  int t = 0;
#pragma unroll 1
  for (; t + 2 < nst; t += 2) {
    waitst(t + 3 <= nst); BAR();
    if (t + 3 < nst) issue(t + 3);
    ldfr(t + 1, afB, bfB); mma(afA, bfA); G4_IL();
    waitst(t + 4 <= nst); BAR();
    if (t + 4 < nst) issue(t + 4);
    ldfr(t + 2, afA, bfA); mma(afB, bfB); G4_IL();
  }
  if (t + 1 < nst) {
    wait_v<0>(); BAR();
    ldfr(t + 1, afB, bfB); mma(afA, bfA); G4_IL();
    mma(afB, bfB);
  } else mma(afA, bfA);
  STAMPK(2, 2);
  __syncthreads();
  f32x4* red = (f32x4*)smem;
  const int rs = (wn * 2 + wm) * 20;
  if (g == 1) {
#pragma unroll
    for (int m = 0; m < 4; ++m)
#pragma unroll
      for (int j = 0; j < NT; ++j) red[(rs + m * 5 + j) * 64 + lane] = acc[m][j];
  }
  __syncthreads();
  if (g == 0) {
    float* dst = slab + (size_t)ks * S * D;
#pragma unroll
    for (int m = 0; m < 4; ++m)
#pragma unroll
      for (int j = 0; j < NT; ++j)
        *(f32x4*)(dst + (size_t)(rt * 128 + wm * 64 + m * 16 + fr) * D + ct * 144 + (wn * 5 + j) * 16 + fq * 4) = acc[m][j] + red[(rs + m * 5 + j) * 64 + lane];
  }
  STAMPK(2, 3);
}
__global__ __launch_bounds__(512, 2) void k_g2(const u16* __restrict__ hbuf, const u16* __restrict__ w2, const u16* __restrict__ o, const u16* __restrict__ wo,
                                               float* __restrict__ slab) {
  __shared__ __attribute__((aligned(16))) char smem[4 * G4_STAGE];
  int bid = blockIdx.x, xcd = bid & 7, idx = bid >> 3;
  int ks = xcd >> 1, rt = (xcd & 1) * 8 + (idx & 7), ct = idx >> 3;
  if (threadIdx.x < 256) g2_body<5>(smem, hbuf, w2, o, wo, slab, ks, rt, ct);
  else g2_body<4>(smem, hbuf, w2, o, wo, slab, ks, rt, ct);
}

__global__ __launch_bounds__(256) void k_l3(const float* __restrict__ slab, const u16* __restrict__ x, u16* __restrict__ out) {
  int i = (blockIdx.x * 256 + threadIdx.x) * 4;
  f32x4 v = *(const f32x4*)(slab + i);
#pragma unroll
  for (int k = 1; k < 4; ++k) v += *(const f32x4*)(slab + (size_t)k * S * D + i);
  uint2 xv = *(const uint2*)(x + i);
  float x0 = __uint_as_float(xv.x << 16), x1 = __uint_as_float(xv.x & 0xffff0000u), x2 = __uint_as_float(xv.y << 16), x3 = __uint_as_float(xv.y & 0xffff0000u);
  *(uint2*)(out + i) = make_uint2(pack2(v[0] + x0, v[1] + x1), pack2(v[2] + x2, v[3] + x3));
}

__global__ __launch_bounds__(256) void k_qo3(const u16* __restrict__ pq, const float* __restrict__ kvslab, u16* __restrict__ o) {
  __shared__ __attribute__((aligned(16))) u16 kvT[32 * 72];
  const int h = blockIdx.x >> 3, rg = blockIdx.x & 7, tid = threadIdx.x, lane = tid & 63, wid = tid >> 6;
  const int fr = lane & 15, fq = lane >> 4;
  const int base = fq * 5 - (fq == 3 ? 1 : 0), cnt = fq < 2 ? 5 : 4;
  float p[4][5];
#pragma unroll
  for (int mt = 0; mt < 4; ++mt) {
    const u16* pr = pq + (size_t)(rg * 256 + wid * 64 + mt * 16 + fr) * D + h * DH + base;
#pragma unroll
    for (int j = 0; j < 5; ++j) p[mt][j] = bf2f(pr[j < cnt ? j : 0]);
  }
  {
    float v[3][16];
#pragma unroll
    for (int i = 0; i < 3; ++i) {
      int e = tid + 256 * i; if (e > 683) e = 683;
      int f = e / 19, n = e - f * 19;
#pragma unroll
      for (int r = 0; r < 16; ++r) v[i][r] = kvslab[((size_t)r * 32 + h) * 720 + f * 20 + n];
    }
    for (int i = tid; i < 32 * 36; i += 256) ((unsigned*)kvT)[i] = 0u;
    __syncthreads();
#pragma unroll
    for (int i = 0; i < 3; ++i) {
      int e = tid + 256 * i;
      float t = 0.f;
#pragma unroll
      for (int r = 0; r < 16; ++r) t += v[i][r];
      if (e < 684) {
        int f = e / 19, n = e - f * 19;
        int s = f >= 18 ? 1 : 0, d = f - 18 * s;
        int q = d < 10 ? d / 5 : 2 + (d - 10) / 4;
        int j = d - (q * 5 - (q == 3 ? 1 : 0));
        kvT[n * 72 + s * 32 + q * 8 + j] = f2bf(t);
      }
    }
  }
  __syncthreads();
  bf16x8 bfr[2][2];
#pragma unroll
  for (int s = 0; s < 2; ++s)
#pragma unroll
    for (int j = 0; j < 2; ++j) bfr[s][j] = *(const bf16x8*)(kvT + (j * 16 + fr) * 72 + s * 32 + fq * 8);
#pragma unroll
  for (int mt = 0; mt < 4; ++mt) {
    float mx = p[mt][0], mn = p[mt][0];
#pragma unroll
    for (int j = 1; j < 5; ++j) { mx = fmaxf(mx, p[mt][j]); mn = fminf(mn, p[mt][j]); }
    mx = fmaxf(mx, __shfl_xor(mx, 16)); mx = fmaxf(mx, __shfl_xor(mx, 32));
    mn = fminf(mn, __shfl_xor(mn, 16)); mn = fminf(mn, __shfl_xor(mn, 32));
    float e1[5], e2[5], s1 = 0.f, s2 = 0.f;
#pragma unroll
    for (int j = 0; j < 5; ++j) {
      e1[j] = j < cnt ? __expf(p[mt][j] - mx) : 0.f; e2[j] = j < cnt ? __expf(mn - p[mt][j]) : 0.f;
      s1 += e1[j]; s2 += e2[j];
    }
    s1 += __shfl_xor(s1, 16); s1 += __shfl_xor(s1, 32);
    s2 += __shfl_xor(s2, 16); s2 += __shfl_xor(s2, 32);
    float r1 = 1.f / s1, r2 = 1.f / s2;
    typedef __attribute__((ext_vector_type(4))) unsigned u32x4;
    u32x4 a0 = {pack2(e1[0] * r1, e1[1] * r1), pack2(e1[2] * r1, e1[3] * r1), pack2(e1[4] * r1, 0.f), 0u};
    u32x4 a1 = {pack2(e2[0] * r2, e2[1] * r2), pack2(e2[2] * r2, e2[3] * r2), pack2(e2[4] * r2, 0.f), 0u};
    bf16x8 af0 = __builtin_bit_cast(bf16x8, a0), af1 = __builtin_bit_cast(bf16x8, a1);
    f32x4 acc[2];
#pragma unroll
    for (int j = 0; j < 2; ++j) {
      acc[j] = f32x4{0.f, 0.f, 0.f, 0.f};
      acc[j] = __builtin_amdgcn_mfma_f32_16x16x32_bf16(bfr[0][j], af0, acc[j], 0, 0, 0);
      acc[j] = __builtin_amdgcn_mfma_f32_16x16x32_bf16(bfr[1][j], af1, acc[j], 0, 0, 0);
    }
    float den = rbf(__shfl(acc[1][2], fr)) + 1e-6f;
    float rd = 1.f / den;
    unsigned* dst = (unsigned*)(o + (size_t)(rg * 256 + wid * 64 + mt * 16 + fr) * D + h * DH);
    dst[2 * fq] = pack2(rbf(acc[0][0]) * rd, rbf(acc[0][1]) * rd);
    dst[2 * fq + 1] = pack2(rbf(acc[0][2]) * rd, rbf(acc[0][3]) * rd);
    if (fq == 0) dst[8] = pack2(rbf(acc[1][0]) * rd, rbf(acc[1][1]) * rd);
  }
}

extern "C" void kernel_launch(void* const* d_in, const int* in_sizes, int n_in, void* d_out, int out_size, void* d_ws, size_t ws_size, hipStream_t stream) {
  const u16* x = (const u16*)d_in[0]; const u16* wqkv = (const u16*)d_in[1]; const u16* wphi = (const u16*)d_in[2]; const u16* wo = (const u16*)d_in[3];
  const u16* w1 = (const u16*)d_in[5]; const u16* w2 = (const u16*)d_in[6];
  u16* out = (u16*)d_out;
  char* ws = (char*)d_ws;
  u16* xn = (u16*)ws; ws += (size_t)S * D * 2;
  u16* qb = (u16*)ws; ws += (size_t)S * D * 2;
  u16* hbuf = (u16*)ws; ws += (size_t)S * F * 2;
  float* kvslab = (float*)ws; ws += 16 * 32 * 720 * 4;
  float* kv = (float*)ws; ws += 32 * 720 * 4;
  u16* o = (u16*)ws; ws += (size_t)S * D * 2;
  float* slab = (float*)ws; ws += (size_t)4 * S * D * 4;
  u16* wf = (u16*)ws; ws += (size_t)D * 2 * D * 2;
  k_ln<<<S / 4 + 144 + 128, 256, 0, stream>>>(x, xn, wqkv, wphi, wf, w1, w2, (unsigned*)kv);
  k_l1<<<256, 512, 0, stream>>>(xn, w1, wqkv, wf, hbuf, qb, kvslab);
  k_qo3<<<256, 256, 0, stream>>>(qb, kvslab, o);
  k_g2<<<256, 512, 0, stream>>>(hbuf, w2, o, wo, slab);
  k_l3<<<S * D / 1024, 256, 0, stream>>>(slab, x, out);
}
```
